```python
import torch

def _softmax(x, axis=-1):
    return torch.softmax(x, dim=axis)
def _gelu(x):
    return torch.nn.functional.gelu(x, approximate="tanh")
def _silu(x):
    return torch.nn.functional.silu(x)
def _sigmoid(x):
    return torch.sigmoid(x)
def _layernorm(x, axis=-1, eps=1e-5):
    m = x.mean(dim=axis, keepdim=True)
    v = x.var(dim=axis, keepdim=True, unbiased=False)
    return (x - m) / torch.sqrt(v + eps)
def _scatter_add(tgt, idx, src, axis=0):
    return tgt.clone().index_add_(axis, idx, src)
def _segment_sum(src, seg, n):
    out = torch.zeros((n,) + src.shape[1:], dtype=src.dtype)
    return out.index_add_(0, seg, src)
def _pad(x, pw):
    flat = tuple(v for pair in reversed(pw) for v in pair)
    return torch.nn.functional.pad(x, flat)

kernel_name = 'standalone__gqa_softmax__near_ridge'

def setup_inputs():

    _w = (lambda a: (lambda t: t.to(torch.bfloat16) if t.is_floating_point() else t)(torch.as_tensor(a)))
    B, Hq, Hkv, Sq, Sk, D = 8, 32, 32, 288, 512, 128
    return {
        "q": _w((lambda *s: torch.randn(*s, dtype=torch.bfloat16) * 0.8)(B, Hq, Sq, D)),
        "k": _w((lambda *s: torch.randn(*s, dtype=torch.bfloat16) * 0.8)(B, Hkv, Sk, D)),
        "v": _w((lambda *s: torch.randn(*s, dtype=torch.bfloat16) * 0.05)(B, Hkv, Sk, D)),
    }

def reference(q, k, v):
    B, Hq, Sq, D = q.shape
    Hkv = k.shape[1]
    rep = Hq // Hkv
    k = (lambda x, n, axis: torch.repeat_interleave(x, n, dim=axis))(k, rep, axis=1)
    v = (lambda x, n, axis: torch.repeat_interleave(x, n, dim=axis))(v, rep, axis=1)
    scale = D ** -0.5
    scores = torch.einsum("bhqd,bhkd->bhqk", q, k) * scale
    p = _softmax(scores, axis=-1)
    return torch.einsum("bhqk,bhkd->bhqd", p, v)
```

```cpp
#include <hip/hip_runtime.h>
#include <hip/hip_bf16.h>
#include <cstdio>

using bf16 = __hip_bfloat16;
constexpr int D = 128, NW = 9, QBLK = 32, KVBLK = 64;
constexpr int B = 8, H = 32, SQ = 288, SK = 512;
constexpr float SCALE = 0.088388347648318440f;
constexpr size_t SHM_V = KVBLK * D * 2, SHM_K = KVBLK * D * 2, SHM_ATTN = SHM_V + SHM_K + NW * 64 * 4;

using bf16x8 = __attribute__((ext_vector_type(8))) short;
using s16x4  = __attribute__((ext_vector_type(4))) short;
using f32x16 = __attribute__((ext_vector_type(16))) float;
using u32x4  = __attribute__((ext_vector_type(4))) unsigned;
#define KSWZ(row, colB) ((row) * 256 + ((colB) ^ (((row) & 7) << 4)))
#define SBAR() __builtin_amdgcn_sched_barrier(0)
__device__ __forceinline__ int crow(int r, int hi) { return (r & 3) + 8 * (r >> 2) + 4 * hi; }
__device__ __forceinline__ unsigned cvtpk(float lo, float hi) {
  unsigned r; asm volatile("v_cvt_pk_bf16_f32 %0, %1, %2" : "=v"(r) : "v"(lo), "v"(hi)); return r;
}
__device__ __forceinline__ int v_st(int k, int c) { const int kk = (k & ~0xC) | ((k & 4) << 1) | ((k & 8) >> 1); return ((kk >> 3) * 4 + (c >> 5)) * 512 + ((kk & 7) * 32 + (c & 31)) * 2; }
__device__ __forceinline__ int v_rd_base(int lane) { return ((lane & 3) << 3) | (((lane >> 2) & 3) << 6) | (((lane >> 4) & 1) << 5) | (((lane >> 5) & 1) << 8); }
constexpr int v_rd_off(int d0, int ks, int half) { return d0 * 512 + ks * 4096 + half * 2048; }
template <int OFF> __device__ __forceinline__ s16x4 tr_read(int vb) {
  s16x4 r; asm volatile("ds_read_b64_tr_b16 %0, %1 offset:%2" : "=&v"(r) : "v"(vb), "i"(OFF) : "memory"); return r;
}
template <int D0> __device__ __forceinline__ void pv_one(f32x16& od, int vb, bf16x8 pa0, bf16x8 pa1, bf16x8 pa2, bf16x8 pa3) {
  const s16x4 l0 = tr_read<v_rd_off(D0, 0, 0)>(vb), h0 = tr_read<v_rd_off(D0, 0, 1)>(vb), l1 = tr_read<v_rd_off(D0, 1, 0)>(vb), h1 = tr_read<v_rd_off(D0, 1, 1)>(vb);
  const s16x4 l2 = tr_read<v_rd_off(D0, 2, 0)>(vb), h2 = tr_read<v_rd_off(D0, 2, 1)>(vb), l3 = tr_read<v_rd_off(D0, 3, 0)>(vb), h3 = tr_read<v_rd_off(D0, 3, 1)>(vb);
  asm volatile("s_waitcnt lgkmcnt(0)" ::: "memory"); SBAR();
#define PK(L, H) (bf16x8){L[0], L[1], L[2], L[3], H[0], H[1], H[2], H[3]}
  od = __builtin_amdgcn_mfma_f32_32x32x16_bf16(pa0, PK(l0, h0), od, 0, 0, 0);
  od = __builtin_amdgcn_mfma_f32_32x32x16_bf16(pa1, PK(l1, h1), od, 0, 0, 0);
  od = __builtin_amdgcn_mfma_f32_32x32x16_bf16(pa2, PK(l2, h2), od, 0, 0, 0);
  od = __builtin_amdgcn_mfma_f32_32x32x16_bf16(pa3, PK(l3, h3), od, 0, 0, 0);
#undef PK
}

__global__ __launch_bounds__(64 * NW, 1)
void attn_base(const bf16* __restrict__ Q, const bf16* __restrict__ K, const bf16* __restrict__ V, bf16* __restrict__ O) {
  extern __shared__ __attribute__((aligned(16))) char lds[];
  const long bh = blockIdx.x;
  const bf16* Qb = Q + bh * SQ * D; const bf16* Kh = K + bh * SK * D; const bf16* Vh = V + bh * SK * D; bf16* Ob = O + bh * SQ * D;
  const int tid = threadIdx.x, wid = tid >> 6, lane = tid & 63, r32 = lane & 31, hi = lane >> 5;
  char* V_lds = lds; char* K_lds = lds + SHM_V;
  float* ws = (float*)(lds + SHM_V + SHM_K) + wid * 64; float* li_l = ws; float* al_l = ws + 32;
  float m_reg = -1e30f, l_reg = 0; f32x16 o[4] = {}; bf16x8 qr[8];
  const bf16* Qw = Qb + (long)(wid * QBLK + r32) * D + hi * 8;
#pragma unroll
  for (int d0 = 0; d0 < 8; ++d0) qr[d0] = *reinterpret_cast<const bf16x8*>(Qw + d0 * 16);
  const int sr = (tid & 511) >> 4, sc = (tid & 15) * 8, vst0 = v_st(sr, sc), vst1 = v_st(32 + sr, sc);
  const int vb0 = (int)(uintptr_t)V_lds + v_rd_base(lane);
  constexpr float C = SCALE * 1.4426950408889634f;
  for (int t = 0; t < SK / KVBLK; ++t) {
    const int k0 = t * KVBLK;
    __syncthreads();
    if (tid < 512) {
      bf16x8 vs0 = *reinterpret_cast<const bf16x8*>(&Vh[(long)(k0 + sr) * D + sc]);
      bf16x8 vs1 = *reinterpret_cast<const bf16x8*>(&Vh[(long)(k0 + 32 + sr) * D + sc]);
      bf16x8 ks0 = *reinterpret_cast<const bf16x8*>(&Kh[(long)(k0 + sr) * D + sc]);
      bf16x8 ks1 = *reinterpret_cast<const bf16x8*>(&Kh[(long)(k0 + 32 + sr) * D + sc]);
      *(bf16x8*)(V_lds + vst0) = vs0; *(bf16x8*)(V_lds + vst1) = vs1;
      int kc = sc * 2;
      *(bf16x8*)(K_lds + KSWZ(sr, kc)) = ks0; *(bf16x8*)(K_lds + KSWZ(32 + sr, kc)) = ks1;
    }
    __syncthreads();
    f32x16 p0 = {}, p1 = {};
#pragma unroll
    for (int d0 = 0; d0 < 8; ++d0) { int cb = (d0 * 16 + hi * 8) * 2;
      bf16x8 b0 = *reinterpret_cast<const bf16x8*>(K_lds + KSWZ(r32, cb));
      bf16x8 b1 = *reinterpret_cast<const bf16x8*>(K_lds + KSWZ(32 + r32, cb));
      p0 = __builtin_amdgcn_mfma_f32_32x32x16_bf16(b0, qr[d0], p0, 0, 0, 0);
      p1 = __builtin_amdgcn_mfma_f32_32x32x16_bf16(b1, qr[d0], p1, 0, 0, 0); }
    float pmax = p0[0];
    for (int r = 1; r < 16; ++r) pmax = fmaxf(pmax, p0[r]);
    for (int r = 0; r < 16; ++r) pmax = fmaxf(pmax, p1[r]);
    { auto rr = __builtin_amdgcn_permlane32_swap(__float_as_uint(pmax), __float_as_uint(pmax), false, false);
      pmax = fmaxf(__uint_as_float(rr[0]), __uint_as_float(rr[1])); }
    float mn = fmaxf(m_reg, pmax); float alpha = __builtin_amdgcn_exp2f((m_reg - mn) * C); m_reg = mn;
    float mnC = -mn * C;
    for (int r = 0; r < 16; ++r) p0[r] = __builtin_amdgcn_exp2f(fmaf(p0[r], C, mnC));
    for (int r = 0; r < 16; ++r) p1[r] = __builtin_amdgcn_exp2f(fmaf(p1[r], C, mnC));
    float ps = 0; for (int r = 0; r < 16; ++r) ps += p0[r]; for (int r = 0; r < 16; ++r) ps += p1[r];
    { auto rr = __builtin_amdgcn_permlane32_swap(__float_as_uint(ps), __float_as_uint(ps), false, false);
      ps = __uint_as_float(rr[0]) + __uint_as_float(rr[1]); }
    l_reg = l_reg * alpha + ps;
    bf16x8 pa0, pa1, pa2, pa3;
#define PK4(P, BASE, OUT) do { unsigned a0 = cvtpk(P[BASE + 0], P[BASE + 1]), a1 = cvtpk(P[BASE + 2], P[BASE + 3]);   \
    unsigned b0 = cvtpk(P[BASE + 4], P[BASE + 5]), b1 = cvtpk(P[BASE + 6], P[BASE + 7]);                              \
    auto r0 = __builtin_amdgcn_permlane32_swap(a0, b0, false, false); auto r1 = __builtin_amdgcn_permlane32_swap(a1, b1, false, false); \
    u32x4 w = {r0[0], r1[0], r0[1], r1[1]}; OUT = *reinterpret_cast<bf16x8*>(&w); } while (0)
    PK4(p0, 0, pa0); PK4(p0, 8, pa1); PK4(p1, 0, pa2); PK4(p1, 8, pa3);
#undef PK4
    if (hi == 0) al_l[r32] = alpha;
    asm volatile("s_waitcnt lgkmcnt(0)" ::: "memory");
#pragma unroll
    for (int d = 0; d < 4; ++d)
#pragma unroll
      for (int r = 0; r < 16; ++r) o[d][r] *= al_l[crow(r, hi)];
    pv_one<0>(o[0], vb0, pa0, pa1, pa2, pa3); pv_one<1>(o[1], vb0, pa0, pa1, pa2, pa3); pv_one<2>(o[2], vb0, pa0, pa1, pa2, pa3); pv_one<3>(o[3], vb0, pa0, pa1, pa2, pa3);
  }
  if (hi == 0) li_l[r32] = l_reg; asm volatile("s_waitcnt lgkmcnt(0)" ::: "memory");
  float rli[16];
#pragma unroll
  for (int r = 0; r < 16; ++r) rli[r] = __builtin_amdgcn_rcpf(li_l[crow(r, hi)]);
  bf16* Ow = Ob + (long)(wid * QBLK) * D;
#pragma unroll
  for (int r = 0; r < 16; ++r) { int orow = crow(r, hi);
#pragma unroll
    for (int d0 = 0; d0 < 4; ++d0) Ow[(long)orow * D + d0 * 32 + r32] = __float2bfloat16(o[d0][r] * rli[r]); }
}

extern "C" void kernel_launch(void* const* d_in, const int* in_sizes, int n_in, void* d_out, int out_size, void* d_ws, size_t ws_size, hipStream_t stream) {
  hipFuncSetAttribute((const void*)attn_base, hipFuncAttributeMaxDynamicSharedMemorySize, (int)SHM_ATTN);
  hipLaunchKernelGGL(attn_base, dim3(B * H), dim3(64 * NW), SHM_ATTN, stream,
                     (const bf16*)d_in[0], (const bf16*)d_in[1], (const bf16*)d_in[2], (bf16*)d_out);
}
```

```cpp
#include <hip/hip_runtime.h>
#include <hip/hip_bf16.h>

namespace {
constexpr int kB = 8, kH = 32, kSQ = 288, kSK = 512, kD = 128;
constexpr int kLds = 163840;
typedef __attribute__((ext_vector_type(8))) short bf16x8;
typedef __attribute__((ext_vector_type(4))) short s16x4;
typedef __attribute__((ext_vector_type(16))) float f32x16;
typedef __attribute__((ext_vector_type(4))) unsigned u32x4;
typedef unsigned short u16;

__device__ __forceinline__ int crow(int r, int hi) { return (r & 3) + 8 * (r >> 2) + 4 * hi; }
__device__ __forceinline__ unsigned cvtpk(float lo, float hi) {
  unsigned r; asm volatile("v_cvt_pk_bf16_f32 %0, %1, %2" : "=v"(r) : "v"(lo), "v"(hi)); return r;
}
__device__ __forceinline__ int kswz(int row, int colB) { return row * 256 + (colB ^ ((row & 7) << 4)); }
__device__ __forceinline__ int v_st(int k, int c) { const int kk = (k & ~0xC) | ((k & 4) << 1) | ((k & 8) >> 1); return ((kk >> 3) * 4 + (c >> 5)) * 512 + ((kk & 7) * 32 + (c & 31)) * 2; }
__device__ __forceinline__ int v_rd_base(int lane) { return ((lane & 3) << 3) | (((lane >> 2) & 3) << 6) | (((lane >> 4) & 1) << 5) | (((lane >> 5) & 1) << 8); }
template <int OFF> __device__ __forceinline__ s16x4 tr_read(int vb) {
  s16x4 r; asm volatile("ds_read_b64_tr_b16 %0, %1 offset:%2" : "=&v"(r) : "v"(vb), "i"(OFF) : "memory"); return r;
}
template <int D0> __device__ __forceinline__ void pv_one(f32x16& od, int vb, bf16x8 pa0, bf16x8 pa1, bf16x8 pa2, bf16x8 pa3) {
  const s16x4 l0 = tr_read<D0 * 512>(vb), h0 = tr_read<D0 * 512 + 2048>(vb), l1 = tr_read<D0 * 512 + 4096>(vb), h1 = tr_read<D0 * 512 + 4096 + 2048>(vb);
  const s16x4 l2 = tr_read<D0 * 512 + 8192>(vb), h2 = tr_read<D0 * 512 + 8192 + 2048>(vb), l3 = tr_read<D0 * 512 + 12288>(vb), h3 = tr_read<D0 * 512 + 12288 + 2048>(vb);
  asm volatile("s_waitcnt lgkmcnt(0)" ::: "memory"); __builtin_amdgcn_sched_barrier(0);
#define PK_(L, H) (bf16x8){L[0], L[1], L[2], L[3], H[0], H[1], H[2], H[3]}
  od = __builtin_amdgcn_mfma_f32_32x32x16_bf16(pa0, PK_(l0, h0), od, 0, 0, 0);
  od = __builtin_amdgcn_mfma_f32_32x32x16_bf16(pa1, PK_(l1, h1), od, 0, 0, 0);
  od = __builtin_amdgcn_mfma_f32_32x32x16_bf16(pa2, PK_(l2, h2), od, 0, 0, 0);
  od = __builtin_amdgcn_mfma_f32_32x32x16_bf16(pa3, PK_(l3, h3), od, 0, 0, 0);
#undef PK_
}
__device__ __forceinline__ void fallback_head(const u16* __restrict__ Qb, const u16* __restrict__ Kh, const u16* __restrict__ Vh, u16* __restrict__ Ob, char* lds) {
  const int tid = threadIdx.x, wid = tid >> 6, lane = tid & 63, r32 = lane & 31, hi = lane >> 5;
  char* V_lds = lds; char* K_lds = lds + 16384;
  float* al_l = (float*)(lds + 32768) + wid * 32;
  const int vb0 = (int)(uintptr_t)V_lds + v_rd_base(lane);
  constexpr float C = 0.088388347648318440f * 1.4426950408889634f;
  for (int round = 0; round < 3; ++round) {
    const int blk = round * 4 + wid;
    const int qblk = blk < 9 ? blk : 8;
    float m_reg = -__builtin_inff(), l_reg = 0.f; f32x16 o[4] = {}; bf16x8 qr[8];
    const u16* Qw = Qb + (qblk * 32 + r32) * kD + hi * 8;
#pragma unroll
    for (int d0 = 0; d0 < 8; ++d0) qr[d0] = *reinterpret_cast<const bf16x8*>(Qw + d0 * 16);
    for (int t = 0; t < kSK / 64; ++t) {
      __syncthreads();
#pragma unroll
      for (int j = 0; j < 4; ++j) {
        const int idx = j * 256 + tid, sr = idx >> 4, sc = (idx & 15) * 8;
        *(bf16x8*)(V_lds + v_st(sr, sc)) = *reinterpret_cast<const bf16x8*>(Vh + (t * 64 + sr) * kD + sc);
        *(bf16x8*)(K_lds + kswz(sr, sc * 2)) = *reinterpret_cast<const bf16x8*>(Kh + (t * 64 + sr) * kD + sc);
      }
      __syncthreads();
      f32x16 p0 = {}, p1 = {};
#pragma unroll
      for (int d0 = 0; d0 < 8; ++d0) { const int cb = (d0 * 16 + hi * 8) * 2;
        const bf16x8 b0 = *reinterpret_cast<const bf16x8*>(K_lds + kswz(r32, cb));
        const bf16x8 b1 = *reinterpret_cast<const bf16x8*>(K_lds + kswz(32 + r32, cb));
        p0 = __builtin_amdgcn_mfma_f32_32x32x16_bf16(b0, qr[d0], p0, 0, 0, 0);
        p1 = __builtin_amdgcn_mfma_f32_32x32x16_bf16(b1, qr[d0], p1, 0, 0, 0); }
      float pmax = p0[0];
#pragma unroll
      for (int r = 1; r < 16; ++r) pmax = fmaxf(pmax, p0[r]);
#pragma unroll
      for (int r = 0; r < 16; ++r) pmax = fmaxf(pmax, p1[r]);
      { auto rr = __builtin_amdgcn_permlane32_swap(__float_as_uint(pmax), __float_as_uint(pmax), false, false);
        pmax = fmaxf(__uint_as_float(rr[0]), __uint_as_float(rr[1])); }
      const float mn = fmaxf(m_reg, pmax);
      const float alpha = __builtin_amdgcn_exp2f((m_reg - mn) * C); m_reg = mn;
      float ps = 0.f;
#pragma unroll
      for (int r = 0; r < 16; ++r) { p0[r] = __builtin_amdgcn_exp2f((p0[r] - mn) * C); ps += p0[r]; }
#pragma unroll
      for (int r = 0; r < 16; ++r) { p1[r] = __builtin_amdgcn_exp2f((p1[r] - mn) * C); ps += p1[r]; }
      { auto rr = __builtin_amdgcn_permlane32_swap(__float_as_uint(ps), __float_as_uint(ps), false, false);
        ps = __uint_as_float(rr[0]) + __uint_as_float(rr[1]); }
      l_reg = l_reg * alpha + ps;
      bf16x8 pa0, pa1, pa2, pa3;
#define PK4_(P, BASE, OUT) do { unsigned a0 = cvtpk(P[BASE + 0], P[BASE + 1]), a1 = cvtpk(P[BASE + 2], P[BASE + 3]);   \
      unsigned b0 = cvtpk(P[BASE + 4], P[BASE + 5]), b1 = cvtpk(P[BASE + 6], P[BASE + 7]);                              \
      auto r0 = __builtin_amdgcn_permlane32_swap(a0, b0, false, false); auto r1 = __builtin_amdgcn_permlane32_swap(a1, b1, false, false); \
      u32x4 w = {r0[0], r1[0], r0[1], r1[1]}; OUT = *reinterpret_cast<bf16x8*>(&w); } while (0)
      PK4_(p0, 0, pa0); PK4_(p0, 8, pa1); PK4_(p1, 0, pa2); PK4_(p1, 8, pa3);
#undef PK4_
      if (hi == 0) al_l[r32] = alpha;
      asm volatile("s_waitcnt lgkmcnt(0)" ::: "memory");
#pragma unroll
      for (int d = 0; d < 4; ++d)
#pragma unroll
        for (int r = 0; r < 16; ++r) o[d][r] *= al_l[crow(r, hi)];
      pv_one<0>(o[0], vb0, pa0, pa1, pa2, pa3); pv_one<1>(o[1], vb0, pa0, pa1, pa2, pa3);
      pv_one<2>(o[2], vb0, pa0, pa1, pa2, pa3); pv_one<3>(o[3], vb0, pa0, pa1, pa2, pa3);
    }
    if (hi == 0) al_l[r32] = l_reg;
    asm volatile("s_waitcnt lgkmcnt(0)" ::: "memory");
    if (blk < 9) {
#pragma unroll
      for (int r = 0; r < 16; ++r) { const int orow = crow(r, hi); const float rl = 1.0f / al_l[orow];
#pragma unroll
        for (int d0 = 0; d0 < 4; ++d0) Ob[(qblk * 32 + orow) * kD + d0 * 32 + r32] = (u16)(cvtpk(o[d0][r] * rl, 0.f) & 0xffffu); }
    }
    asm volatile("s_waitcnt lgkmcnt(0)" ::: "memory");
  }
}
}

__global__ __launch_bounds__(256, 1)
void gqa_softmax_attn(const u16* __restrict__ Q, const u16* __restrict__ K, const u16* __restrict__ V, u16* __restrict__ O) {
  extern __shared__ __attribute__((aligned(16))) char lds[];
  const unsigned bh = blockIdx.x;
  unsigned flag;
  asm volatile(
    "v_lshrrev_b32 v1, 6, v0\n"
    "v_and_b32 v5, 63, v0\n"
    "v_readfirstlane_b32 s36, v1\n"
    "s_getpc_b64 s[60:61]\n"
    "v_lshlrev_b32 v210, 6, v0\n"
    "s_nop 2\n"
    "global_load_dword v208, v210, s[60:61]\n"
    "v_lshlrev_b32 v211, 4, v0\n"
    "s_add_u32 s60, s60, 16384\n"
    "s_addc_u32 s61, s61, 0\n"
    "global_load_dword v209, v211, s[60:61] offset:0\n"
    "s_mul_i32 s54, %[bh], 73728\n"
    "s_lshl_b32 s55, %[bh], 17\n"
    "s_add_u32 s24, %[klo], s55\n"
    "s_addc_u32 s25, %[khi], 0\n"
    "s_and_b32 s25, s25, 0xffff\n"
    "s_mov_b32 s26, 131072\n"
    "s_mov_b32 s27, 0x20000\n"
    "s_add_u32 s20, %[qlo], s54\n"
    "s_addc_u32 s21, %[qhi], 0\n"
    "s_and_b32 s21, s21, 0xffff\n"
    "s_mov_b32 s22, 73728\n"
    "s_mov_b32 s23, 0x20000\n"
    "s_mov_b32 s40, 0\n"
    "s_add_u32 s28, %[vlo], s55\n"
    "s_addc_u32 s29, %[vhi], 0\n"
    "s_and_b32 s29, s29, 0xffff\n"
    "s_mov_b32 s30, 131072\n"
    "s_mov_b32 s31, 0x20000\n"
    "s_mov_b32 s41, 16384\n"
    "v_lshrrev_b32 v1, 3, v5\n"
    "v_and_b32 v2, 7, v5\n"
    "v_and_b32 v3, 6, v1\n"
    "s_lshl_b32 s56, s36, 12\n"
    "s_add_u32 s37, %[lds], s56\n"
    "v_xor_b32 v2, v2, v3\n"
    "v_lshlrev_b32 v2, 4, v2\n"
    "v_lshl_add_u32 v2, v1, 8, v2\n"
    "v_add_u32 v236, s56, v2\n"
    "v_add_u32 v237, 128, v236\n"
    "v_and_b32 v6, 15, v5\n"
    "v_lshrrev_b32 v7, 4, v5\n"
    "s_mul_i32 s12, s36, 12288\n"
    "s_add_u32 s13, s12, 4096\n"
    "s_add_u32 s14, s12, 8192\n"
    "s_add_u32 s15, s12, 12288\n"
    "s_lshr_b32 s58, s36, 1\n"
    "s_lshl_b32 s58, s58, 12\n"
    "s_add_u32 s58, s58, 65536\n"
    "s_sub_u32 s16, s58, s56\n"
    "s_lshl_b32 s57, s36, 14\n"
    "s_add_u32 m0, s37, 0\n"
    "s_nop 0\n"
    "buffer_load_dwordx4 v236, s[24:27], s40 offen sc1 lds\n"
    "buffer_load_dwordx4 v236, s[24:27], s40 offen offset:2048 sc1 lds\n"
    "s_add_u32 m0, s37, 1024\n"
    "s_nop 0\n"
    "buffer_load_dwordx4 v237, s[24:27], s40 offen sc1 lds\n"
    "buffer_load_dwordx4 v237, s[24:27], s40 offen offset:2048 sc1 lds\n"
    "s_add_u32 m0, s37, 32768\n"
    "s_nop 0\n"
    "buffer_load_dwordx4 v236, s[20:23], s12 offen sc1 lds\n"
    "buffer_load_dwordx4 v236, s[20:23], s12 offen offset:2048 sc1 lds\n"
    "s_add_u32 m0, s37, 33792\n"
    "s_nop 0\n"
    "buffer_load_dwordx4 v237, s[20:23], s12 offen sc1 lds\n"
    "buffer_load_dwordx4 v237, s[20:23], s12 offen offset:2048 sc1 lds\n"
    "s_add_u32 m0, s37, 49152\n"
    "s_nop 0\n"
    "buffer_load_dwordx4 v236, s[20:23], s13 offen sc1 lds\n"
    "buffer_load_dwordx4 v236, s[20:23], s13 offen offset:2048 sc1 lds\n"
    "s_add_u32 m0, s37, 50176\n"
    "s_nop 0\n"
    "buffer_load_dwordx4 v237, s[20:23], s13 offen sc1 lds\n"
    "buffer_load_dwordx4 v237, s[20:23], s13 offen offset:2048 sc1 lds\n"
    "s_add_u32 m0, s37, 114688\n"
    "s_nop 0\n"
    "buffer_load_dwordx4 v236, s[20:23], s16 offen sc1 lds\n"
    "buffer_load_dwordx4 v236, s[20:23], s16 offen offset:2048 sc1 lds\n"
    "s_add_u32 m0, s37, 115712\n"
    "s_nop 0\n"
    "buffer_load_dwordx4 v237, s[20:23], s16 offen sc1 lds\n"
    "buffer_load_dwordx4 v237, s[20:23], s16 offen offset:2048 sc1 lds\n"
    "s_add_u32 s64, %[olo], s54\n"
    "s_addc_u32 s65, %[ohi], 0\n"
    "s_and_b32 s65, s65, 0xffff\n"
    "s_mov_b32 s66, 73728\n"
    "s_mov_b32 s67, 0x20000\n"
    "v_and_b32 v212, 3, v5\n"
    "v_lshlrev_b32 v213, 14, v5\n"
    "v_cmp_eq_u32 vcc, s36, v212\n"
    "v_mov_b32 v212, 0x7ffffff0\n"
    "s_nop 1\n"
    "v_cndmask_b32 v213, v212, v213, vcc\n"
    "buffer_load_dword v214, v213, s[64:67], 0 offen\n"
    "buffer_load_dword v215, v213, s[28:31], 0 offen\n"
    "buffer_load_dword v216, v213, s[24:27], 0 offen\n"
    "buffer_load_dword v217, v213, s[20:23], 0 offen\n"
    "s_mov_b32 s42, 32768\n"
    "s_mov_b32 s43, 49152\n"
    "s_mov_b32 s44, 65536\n"
    "s_mov_b32 s48, 0\n"
    "s_mov_b32 s49, 4096\n"
    "s_mov_b32 s50, 8192\n"
    "s_mov_b32 s51, 12288\n"
    "s_add_u32 s68, s37, 65536\n"
    "s_add_u32 s69, s37, 16384\n"
    "s_add_u32 s70, s37, 81920\n"
    "s_add_u32 s71, s37, 32768\n"
    "s_mov_b32 s72, 65536\n"
    "s_mov_b32 s73, 81920\n"
    "s_mov_b32 s74, 98304\n"
    "s_mov_b32 s75, 3\n"
    "v_mov_b64 v[220:221], 0\n"
    "v_mov_b64 v[222:223], 0\n"
    "s_nop 1\n"
    "v_mfma_f32_16x16x32_bf16 a[0:3], v[220:223], v[220:223], 0\n"
    "v_mfma_f32_16x16x32_bf16 a[4:7], v[220:223], v[220:223], 0\n"
    "v_mfma_f32_16x16x32_bf16 a[8:11], v[220:223], v[220:223], 0\n"
    "v_mfma_f32_16x16x32_bf16 a[12:15], v[220:223], v[220:223], 0\n"
    "v_mfma_f32_16x16x32_bf16 a[16:19], v[220:223], v[220:223], 0\n"
    "v_mfma_f32_16x16x32_bf16 a[20:23], v[220:223], v[220:223], 0\n"
    "v_mfma_f32_16x16x32_bf16 a[24:27], v[220:223], v[220:223], 0\n"
    "v_mfma_f32_16x16x32_bf16 a[28:31], v[220:223], v[220:223], 0\n"
    "v_mfma_f32_16x16x32_bf16 a[208:211], v[220:223], v[220:223], 0\n"
    "v_mfma_f32_16x16x32_bf16 a[212:215], v[220:223], v[220:223], 0\n"
    "v_mfma_f32_16x16x32_bf16 a[216:219], v[220:223], v[220:223], 0\n"
    "v_mfma_f32_16x16x32_bf16 a[220:223], v[220:223], v[220:223], 0\n"
    "v_mfma_f32_16x16x32_bf16 a[224:227], v[220:223], v[220:223], 0\n"
    "s_and_b32 s59, s36, 1\n"
    "s_lshl_b32 s60, s59, 13\n"
    "s_xor_b32 s61, s60, 8192\n"
    "s_add_u32 s60, s60, %[lds]\n"
    "s_add_u32 s61, s61, %[lds]\n"
    "v_lshrrev_b32 v1, 3, v6\n"
    "v_lshlrev_b32 v1, 11, v1\n"
    "v_and_b32 v2, 7, v6\n"
    "v_lshl_add_u32 v1, v2, 7, v1\n"
    "v_and_b32 v2, 6, v6\n"
    "v_xor_b32 v3, v7, v2\n"
    "v_xor_b32 v4, 4, v3\n"
    "v_lshl_add_u32 v3, v3, 4, v1\n"
    "v_lshl_add_u32 v4, v4, 4, v1\n"
    "v_add_u32 v224, s60, v3\n"
    "v_add_u32 v225, s60, v4\n"
    "v_add_u32 v232, 0x8000, v224\n"
    "v_add_u32 v233, 0x8000, v225\n"
    "v_add_u32 v226, s61, v3\n"
    "v_add_u32 v227, s61, v4\n"
    "v_add_u32 v234, 0x8000, v226\n"
    "v_add_u32 v235, 0x8000, v227\n"
    "v_add_u32 v248, s37, v3\n"
    "v_add_u32 v249, s37, v4\n"
    "v_add_u32 v250, 98304, v248\n"
    "v_add_u32 v251, 98304, v249\n"
    "v_lshrrev_b32 v1, 2, v5\n"
    "v_and_b32 v1, 7, v1\n"
    "v_lshrrev_b32 v2, 5, v5\n"
    "v_lshlrev_b32 v2, 11, v2\n"
    "v_lshl_add_u32 v2, v1, 7, v2\n"
    "v_add_u32 v2, 65536, v2\n"
    "v_and_b32 v1, 6, v1\n"
    "v_and_b32 v3, 3, v5\n"
    "v_xor_b32 v3, v3, v1\n"
    "v_xor_b32 v4, 0, v3\n"
    "v_lshl_add_u32 v4, v4, 4, v2\n"
    "v_add_u32 v228, s60, v4\n"
    "v_add_u32 v230, s61, v4\n"
    "v_xor_b32 v4, 4, v3\n"
    "v_lshl_add_u32 v4, v4, 4, v2\n"
    "v_add_u32 v229, s60, v4\n"
    "v_add_u32 v231, s61, v4\n"
    "v_lshlrev_b32 v1, 4, v7\n"
    "v_lshl_add_u32 v1, v6, 8, v1\n"
    "v_add_u32 v240, s57, v1\n"
    "s_lshl_b32 s62, s59, 7\n"
    "s_add_u32 s62, s62, s58\n"
    "v_add_u32 v241, s62, v1\n"
    "s_mul_i32 s62, s36, 9216\n"
    "s_add_u32 s62, s62, %[lds]\n"
    "s_xor_b32 s63, s36, 1\n"
    "s_mul_i32 s63, s63, 9216\n"
    "s_add_u32 s63, s63, %[lds]\n"
    "v_lshlrev_b32 v1, 4, v5\n"
    "v_add_u32 v242, s62, v1\n"
    "v_add_u32 v245, s63, v1\n"
    "s_lshl_b32 s59, s59, 12\n"
    "v_add_u32 v243, s59, v242\n"
    "v_add_u32 v244, s59, v245\n"
    "v_mov_b32 v1, 0x3f803f80\n"
    "v_accvgpr_write_b32 a228, v1\n"
    "v_accvgpr_write_b32 a229, v1\n"
    "v_accvgpr_write_b32 a230, v1\n"
    "v_accvgpr_write_b32 a231, v1\n"
    "v_mov_b32 v246, 0x2b800000\n"
    "v_mov_b32 v247, 0x53800000\n"
    "s_mov_b64 s[52:53], 0\n"
    "s_lshl_b32 s62, s36, 2\n"
    "s_add_u32 s63, %[lds], 40960\n"
    "s_add_u32 s62, s62, s63\n"
    "s_mov_b32 s56, 0x3e0293ee\n"
    "s_mov_b32 s57, 0xffff0000\n"
    "v_add_u32 v4, 0x8000, v228\n"
    "v_add_u32 v5, 0x8000, v229\n"
    "v_add_u32 v6, 0x8000, v230\n"
    "v_add_u32 v7, 0x8000, v231\n"
    "s_waitcnt vmcnt(16)\n"
    "s_barrier\n"
    "s_waitcnt vmcnt(12)\n"
    "ds_read_b128 v[8:11], v248 offset:32768\n"
    "ds_read_b128 v[12:15], v249 offset:32768\n"
    "ds_read_b128 v[16:19], v248 offset:33792\n"
    "ds_read_b128 v[20:23], v249 offset:33792\n"
    "s_add_u32 m0, s37, 131072\n"
    "ds_read_b128 a[112:115], v224 offset:0\n"
    "ds_read_b128 a[116:119], v225 offset:0\n"
    "ds_read_b128 a[120:123], v224 offset:1024\n"
    "ds_read_b128 a[124:127], v225 offset:1024\n"
    "buffer_load_dwordx4 v236, s[20:23], s14 offen sc1 lds\n"
    "buffer_load_dwordx4 v236, s[20:23], s14 offen offset:2048 sc1 lds\n"
    "s_add_u32 m0, s37, 132096\n"
    "ds_read_b128 a[128:131], v224 offset:4096\n"
    "ds_read_b128 a[132:135], v225 offset:4096\n"
    "ds_read_b128 a[136:139], v224 offset:5120\n"
    "ds_read_b128 a[140:143], v225 offset:5120\n"
    "buffer_load_dwordx4 v237, s[20:23], s14 offen sc1 lds\n"
    "buffer_load_dwordx4 v237, s[20:23], s14 offen offset:2048 sc1 lds\n"
    "s_waitcnt lgkmcnt(11)\n"
    "v_lshlrev_b32 v1, 16, v8\n"
    "v_and_b32 v2, s57, v8\n"
    "v_lshlrev_b32 v3, 16, v9\n"
    "v_and_b32 v238, s57, v9\n"
    "v_mul_f32 v1, s56, v1\n"
    "v_mul_f32 v2, s56, v2\n"
    "v_mul_f32 v3, s56, v3\n"
    "v_mul_f32 v238, s56, v238\n"
    "v_cvt_pk_bf16_f32 v1, v1, v2\n"
    "v_cvt_pk_bf16_f32 v3, v3, v238\n"
    "v_accvgpr_write_b32 a32, v1\n"
    "v_accvgpr_write_b32 a33, v3\n"
    "v_lshlrev_b32 v239, 16, v10\n"
    "v_and_b32 v254, s57, v10\n"
    "v_lshlrev_b32 v255, 16, v11\n"
    "v_and_b32 v253, s57, v11\n"
    "v_mul_f32 v239, s56, v239\n"
    "v_mul_f32 v254, s56, v254\n"
    "v_mul_f32 v255, s56, v255\n"
    "v_mul_f32 v253, s56, v253\n"
    "v_cvt_pk_bf16_f32 v239, v239, v254\n"
    "v_cvt_pk_bf16_f32 v255, v255, v253\n"
    "v_accvgpr_write_b32 a34, v239\n"
    "v_accvgpr_write_b32 a35, v255\n"
    "s_waitcnt lgkmcnt(10)\n"
    "v_lshlrev_b32 v1, 16, v12\n"
    "v_and_b32 v2, s57, v12\n"
    "v_lshlrev_b32 v3, 16, v13\n"
    "v_and_b32 v238, s57, v13\n"
    "v_mul_f32 v1, s56, v1\n"
    "v_mul_f32 v2, s56, v2\n"
    "v_mul_f32 v3, s56, v3\n"
    "v_mul_f32 v238, s56, v238\n"
    "v_cvt_pk_bf16_f32 v1, v1, v2\n"
    "v_cvt_pk_bf16_f32 v3, v3, v238\n"
    "v_accvgpr_write_b32 a36, v1\n"
    "v_accvgpr_write_b32 a37, v3\n"
    "v_lshlrev_b32 v239, 16, v14\n"
    "v_and_b32 v254, s57, v14\n"
    "v_lshlrev_b32 v255, 16, v15\n"
    "v_and_b32 v253, s57, v15\n"
    "v_mul_f32 v239, s56, v239\n"
    "v_mul_f32 v254, s56, v254\n"
    "v_mul_f32 v255, s56, v255\n"
    "v_mul_f32 v253, s56, v253\n"
    "v_cvt_pk_bf16_f32 v239, v239, v254\n"
    "v_cvt_pk_bf16_f32 v255, v255, v253\n"
    "v_accvgpr_write_b32 a38, v239\n"
    "v_accvgpr_write_b32 a39, v255\n"
    "s_waitcnt lgkmcnt(9)\n"
    "v_lshlrev_b32 v1, 16, v16\n"
    "v_and_b32 v2, s57, v16\n"
    "v_lshlrev_b32 v3, 16, v17\n"
    "v_and_b32 v238, s57, v17\n"
    "v_mul_f32 v1, s56, v1\n"
    "v_mul_f32 v2, s56, v2\n"
    "v_mul_f32 v3, s56, v3\n"
    "v_mul_f32 v238, s56, v238\n"
    "v_cvt_pk_bf16_f32 v1, v1, v2\n"
    "v_cvt_pk_bf16_f32 v3, v3, v238\n"
    "v_accvgpr_write_b32 a40, v1\n"
    "v_accvgpr_write_b32 a41, v3\n"
    "v_lshlrev_b32 v239, 16, v18\n"
    "v_and_b32 v254, s57, v18\n"
    "v_lshlrev_b32 v255, 16, v19\n"
    "v_and_b32 v253, s57, v19\n"
    "v_mul_f32 v239, s56, v239\n"
    "v_mul_f32 v254, s56, v254\n"
    "v_mul_f32 v255, s56, v255\n"
    "v_mul_f32 v253, s56, v253\n"
    "v_cvt_pk_bf16_f32 v239, v239, v254\n"
    "v_cvt_pk_bf16_f32 v255, v255, v253\n"
    "v_accvgpr_write_b32 a42, v239\n"
    "v_accvgpr_write_b32 a43, v255\n"
    "s_waitcnt lgkmcnt(8)\n"
    "v_lshlrev_b32 v1, 16, v20\n"
    "v_and_b32 v2, s57, v20\n"
    "v_lshlrev_b32 v3, 16, v21\n"
    "v_and_b32 v238, s57, v21\n"
    "v_mul_f32 v1, s56, v1\n"
    "v_mul_f32 v2, s56, v2\n"
    "v_mul_f32 v3, s56, v3\n"
    "v_mul_f32 v238, s56, v238\n"
    "v_cvt_pk_bf16_f32 v1, v1, v2\n"
    "v_cvt_pk_bf16_f32 v3, v3, v238\n"
    "v_accvgpr_write_b32 a44, v1\n"
    "v_accvgpr_write_b32 a45, v3\n"
    "v_lshlrev_b32 v239, 16, v22\n"
    "v_and_b32 v254, s57, v22\n"
    "v_lshlrev_b32 v255, 16, v23\n"
    "v_and_b32 v253, s57, v23\n"
    "v_mul_f32 v239, s56, v239\n"
    "v_mul_f32 v254, s56, v254\n"
    "v_mul_f32 v255, s56, v255\n"
    "v_mul_f32 v253, s56, v253\n"
    "v_cvt_pk_bf16_f32 v239, v239, v254\n"
    "v_cvt_pk_bf16_f32 v255, v255, v253\n"
    "v_accvgpr_write_b32 a46, v239\n"
    "v_accvgpr_write_b32 a47, v255\n"
    "s_waitcnt lgkmcnt(7)\n"
    "v_mfma_f32_16x16x32_bf16 v[208:211], a[112:115], a[32:35], 0\n"
    "s_waitcnt lgkmcnt(0)\n"
    "ds_read_b128 a[144:147], v226 offset:0\n"
    "ds_read_b128 a[148:151], v227 offset:0\n"
    "v_mfma_f32_16x16x32_bf16 v[208:211], a[116:119], a[36:39], v[208:211]\n"
    "ds_read_b128 a[152:155], v226 offset:1024\n"
    "ds_read_b128 a[156:159], v227 offset:1024\n"
    "v_mfma_f32_16x16x32_bf16 v[208:211], a[120:123], a[40:43], v[208:211]\n"
    "ds_read_b128 a[160:163], v226 offset:4096\n"
    "ds_read_b128 a[164:167], v227 offset:4096\n"
    "v_mfma_f32_16x16x32_bf16 v[208:211], a[124:127], a[44:47], v[208:211]\n"
    "ds_read_b128 a[168:171], v226 offset:5120\n"
    "ds_read_b128 a[172:175], v227 offset:5120\n"
    "s_waitcnt vmcnt(6)\n"
    "v_mfma_f32_16x16x32_bf16 v[212:215], a[128:131], a[32:35], 0\n"
    "v_mov_b64 v[8:9], 0\n"
    "v_mov_b64 v[10:11], 0\n"
    "v_mfma_f32_16x16x32_bf16 v[212:215], a[132:135], a[36:39], v[212:215]\n"
    "v_mov_b64 v[12:13], 0\n"
    "v_mov_b64 v[14:15], 0\n"
    "v_mfma_f32_16x16x32_bf16 v[212:215], a[136:139], a[40:43], v[212:215]\n"
    "v_mov_b64 v[16:17], 0\n"
    "v_mov_b64 v[18:19], 0\n"
    "v_mfma_f32_16x16x32_bf16 v[212:215], a[140:143], a[44:47], v[212:215]\n"
    "v_exp_f32 v208, v208\n"
    "s_waitcnt vmcnt(4) lgkmcnt(7)\n"
    "v_mfma_f32_16x16x32_bf16 v[216:219], a[144:147], a[32:35], 0\n"
    "s_waitcnt lgkmcnt(0)\n"
    "v_exp_f32 v209, v209\n"
    "v_mfma_f32_16x16x32_bf16 v[216:219], a[148:151], a[36:39], v[216:219]\n"
    "v_exp_f32 v210, v210\n"
    "v_mfma_f32_16x16x32_bf16 v[216:219], a[152:155], a[40:43], v[216:219]\n"
    "v_cvt_pk_bf16_f32 v136, v208, v209\n"
    "v_mov_b64 v[20:21], 0\n"
    "v_mfma_f32_16x16x32_bf16 v[216:219], a[156:159], a[44:47], v[216:219]\n"
    "v_exp_f32 v211, v211\n"
    "v_mfma_f32_16x16x32_bf16 v[220:223], a[160:163], a[32:35], 0\n"
    "v_cvt_pk_bf16_f32 v137, v210, v211\n"
    "v_mov_b64 v[22:23], 0\n"
    "v_mfma_f32_16x16x32_bf16 v[220:223], a[164:167], a[36:39], v[220:223]\n"
    "v_exp_f32 v212, v212\n"
    "v_mfma_f32_16x16x32_bf16 v[220:223], a[168:171], a[40:43], v[220:223]\n"
    "v_exp_f32 v213, v213\n"
    "v_mfma_f32_16x16x32_bf16 v[220:223], a[172:175], a[44:47], v[220:223]\n"
    "ds_read_b128 v[40:43], v248 offset:49152\n"
    "ds_read_b128 v[44:47], v249 offset:49152\n"
    "s_add_u32 m0, s37, 147456\n"
    "ds_read_b128 v[48:51], v248 offset:50176\n"
    "ds_read_b128 v[52:55], v249 offset:50176\n"
    "v_exp_f32 v214, v214\n"
    "buffer_load_dwordx4 v236, s[20:23], s15 offen sc1 lds\n"
    "buffer_load_dwordx4 v236, s[20:23], s15 offen offset:2048 sc1 lds\n"
    "s_add_u32 m0, s37, 148480\n"
    "v_cvt_pk_bf16_f32 v138, v212, v213\n"
    "v_exp_f32 v215, v215\n"
    "v_mov_b64 v[24:25], 0\n"
    "buffer_load_dwordx4 v237, s[20:23], s15 offen sc1 lds\n"
    "buffer_load_dwordx4 v237, s[20:23], s15 offen offset:2048 sc1 lds\n"
    "s_waitcnt lgkmcnt(3)\n"
    "v_lshlrev_b32 v1, 16, v40\n"
    "v_and_b32 v2, s57, v40\n"
    "v_lshlrev_b32 v3, 16, v41\n"
    "v_and_b32 v238, s57, v41\n"
    "v_mul_f32 v1, s56, v1\n"
    "v_mul_f32 v2, s56, v2\n"
    "v_mul_f32 v3, s56, v3\n"
    "v_mul_f32 v238, s56, v238\n"
    "v_cvt_pk_bf16_f32 v1, v1, v2\n"
    "v_cvt_pk_bf16_f32 v3, v3, v238\n"
    "v_accvgpr_write_b32 a48, v1\n"
    "v_accvgpr_write_b32 a49, v3\n"
    "v_lshlrev_b32 v239, 16, v42\n"
    "v_and_b32 v254, s57, v42\n"
    "v_lshlrev_b32 v255, 16, v43\n"
    "v_and_b32 v253, s57, v43\n"
    "v_mul_f32 v239, s56, v239\n"
    "v_mul_f32 v254, s56, v254\n"
    "v_mul_f32 v255, s56, v255\n"
    "v_mul_f32 v253, s56, v253\n"
    "v_cvt_pk_bf16_f32 v239, v239, v254\n"
    "v_cvt_pk_bf16_f32 v255, v255, v253\n"
    "v_accvgpr_write_b32 a50, v239\n"
    "v_accvgpr_write_b32 a51, v255\n"
    "s_waitcnt lgkmcnt(2)\n"
    "v_lshlrev_b32 v1, 16, v44\n"
    "v_and_b32 v2, s57, v44\n"
    "v_lshlrev_b32 v3, 16, v45\n"
    "v_and_b32 v238, s57, v45\n"
    "v_mul_f32 v1, s56, v1\n"
    "v_mul_f32 v2, s56, v2\n"
    "v_mul_f32 v3, s56, v3\n"
    "v_mul_f32 v238, s56, v238\n"
    "v_cvt_pk_bf16_f32 v1, v1, v2\n"
    "v_cvt_pk_bf16_f32 v3, v3, v238\n"
    "v_accvgpr_write_b32 a52, v1\n"
    "v_accvgpr_write_b32 a53, v3\n"
    "v_lshlrev_b32 v239, 16, v46\n"
    "v_and_b32 v254, s57, v46\n"
    "v_lshlrev_b32 v255, 16, v47\n"
    "v_and_b32 v253, s57, v47\n"
    "v_mul_f32 v239, s56, v239\n"
    "v_mul_f32 v254, s56, v254\n"
    "v_mul_f32 v255, s56, v255\n"
    "v_mul_f32 v253, s56, v253\n"
    "v_cvt_pk_bf16_f32 v239, v239, v254\n"
    "v_cvt_pk_bf16_f32 v255, v255, v253\n"
    "v_accvgpr_write_b32 a54, v239\n"
    "v_accvgpr_write_b32 a55, v255\n"
    "s_waitcnt lgkmcnt(1)\n"
    "v_lshlrev_b32 v1, 16, v48\n"
    "v_and_b32 v2, s57, v48\n"
    "v_lshlrev_b32 v3, 16, v49\n"
    "v_and_b32 v238, s57, v49\n"
    "v_mul_f32 v1, s56, v1\n"
    "v_mul_f32 v2, s56, v2\n"
    "v_mul_f32 v3, s56, v3\n"
    "v_mul_f32 v238, s56, v238\n"
    "v_cvt_pk_bf16_f32 v1, v1, v2\n"
    "v_cvt_pk_bf16_f32 v3, v3, v238\n"
    "v_accvgpr_write_b32 a56, v1\n"
    "v_accvgpr_write_b32 a57, v3\n"
    "v_lshlrev_b32 v239, 16, v50\n"
    "v_and_b32 v254, s57, v50\n"
    "v_lshlrev_b32 v255, 16, v51\n"
    "v_and_b32 v253, s57, v51\n"
    "v_mul_f32 v239, s56, v239\n"
    "v_mul_f32 v254, s56, v254\n"
    "v_mul_f32 v255, s56, v255\n"
    "v_mul_f32 v253, s56, v253\n"
    "v_cvt_pk_bf16_f32 v239, v239, v254\n"
    "v_cvt_pk_bf16_f32 v255, v255, v253\n"
    "v_accvgpr_write_b32 a58, v239\n"
    "v_accvgpr_write_b32 a59, v255\n"
    "s_waitcnt lgkmcnt(0)\n"
    "v_lshlrev_b32 v1, 16, v52\n"
    "v_and_b32 v2, s57, v52\n"
    "v_lshlrev_b32 v3, 16, v53\n"
    "v_and_b32 v238, s57, v53\n"
    "v_mul_f32 v1, s56, v1\n"
    "v_mul_f32 v2, s56, v2\n"
    "v_mul_f32 v3, s56, v3\n"
    "v_mul_f32 v238, s56, v238\n"
    "v_cvt_pk_bf16_f32 v1, v1, v2\n"
    "v_cvt_pk_bf16_f32 v3, v3, v238\n"
    "v_accvgpr_write_b32 a60, v1\n"
    "v_accvgpr_write_b32 a61, v3\n"
    "v_lshlrev_b32 v239, 16, v54\n"
    "v_and_b32 v254, s57, v54\n"
    "v_lshlrev_b32 v255, 16, v55\n"
    "v_and_b32 v253, s57, v55\n"
    "v_mul_f32 v239, s56, v239\n"
    "v_mul_f32 v254, s56, v254\n"
    "v_mul_f32 v255, s56, v255\n"
    "v_mul_f32 v253, s56, v253\n"
    "v_cvt_pk_bf16_f32 v239, v239, v254\n"
    "v_cvt_pk_bf16_f32 v255, v255, v253\n"
    "v_accvgpr_write_b32 a62, v239\n"
    "v_accvgpr_write_b32 a63, v255\n"
    "v_mfma_f32_16x16x32_bf16 v[208:211], a[112:115], a[48:51], 0\n"
    "v_cvt_pk_bf16_f32 v139, v214, v215\n"
    "v_mov_b64 v[26:27], 0\n"
    "v_mfma_f32_16x16x32_bf16 v[208:211], a[116:119], a[52:55], v[208:211]\n"
    "v_exp_f32 v216, v216\n"
    "v_mfma_f32_16x16x32_bf16 v[208:211], a[120:123], a[56:59], v[208:211]\n"
    "v_exp_f32 v217, v217\n"
    "v_mfma_f32_16x16x32_bf16 v[208:211], a[124:127], a[60:63], v[208:211]\n"
    "v_exp_f32 v218, v218\n"
    "v_mfma_f32_16x16x32_bf16 v[212:215], a[128:131], a[48:51], 0\n"
    "v_cvt_pk_bf16_f32 v140, v216, v217\n"
    "v_mov_b64 v[28:29], 0\n"
    "v_mfma_f32_16x16x32_bf16 v[212:215], a[132:135], a[52:55], v[212:215]\n"
    "v_exp_f32 v219, v219\n"
    "v_mfma_f32_16x16x32_bf16 v[212:215], a[136:139], a[56:59], v[212:215]\n"
    "v_cvt_pk_bf16_f32 v141, v218, v219\n"
    "v_mov_b64 v[30:31], 0\n"
    "v_mfma_f32_16x16x32_bf16 v[212:215], a[140:143], a[60:63], v[212:215]\n"
    "v_exp_f32 v220, v220\n"
    "v_mfma_f32_16x16x32_bf16 v[216:219], a[144:147], a[48:51], 0\n"
    "v_exp_f32 v221, v221\n"
    "v_mfma_f32_16x16x32_bf16 v[216:219], a[148:151], a[52:55], v[216:219]\n"
    "v_exp_f32 v222, v222\n"
    "v_mfma_f32_16x16x32_bf16 v[216:219], a[152:155], a[56:59], v[216:219]\n"
    "v_cvt_pk_bf16_f32 v142, v220, v221\n"
    "v_mov_b64 v[32:33], 0\n"
    "v_mfma_f32_16x16x32_bf16 v[216:219], a[156:159], a[60:63], v[216:219]\n"
    "v_exp_f32 v223, v223\n"
    "s_nop 0\n"
    "v_cvt_pk_bf16_f32 v143, v222, v223\n"
    "v_exp_f32 v208, v208\n"
    "v_mov_b64 v[34:35], 0\n"
    "v_mfma_f32_16x16x32_bf16 v[220:223], a[160:163], a[48:51], 0\n"
    "v_exp_f32 v209, v209\n"
    "v_mfma_f32_16x16x32_bf16 v[220:223], a[164:167], a[52:55], v[220:223]\n"
    "v_exp_f32 v210, v210\n"
    "v_mfma_f32_16x16x32_bf16 v[220:223], a[168:171], a[56:59], v[220:223]\n"
    "v_cvt_pk_bf16_f32 v144, v208, v209\n"
    "v_mov_b64 v[36:37], 0\n"
    "v_mfma_f32_16x16x32_bf16 v[220:223], a[172:175], a[60:63], v[220:223]\n"
    "ds_read_b128 v[188:191], v250 offset:16384\n"
    "ds_read_b128 v[192:195], v251 offset:16384\n"
    "s_add_u32 m0, s37, 65536\n"
    "ds_read_b128 v[196:199], v250 offset:17408\n"
    "ds_read_b128 v[200:203], v251 offset:17408\n"
    "v_exp_f32 v211, v211\n"
    "buffer_load_dwordx4 v236, s[28:31], s40 offen sc1 lds\n"
    "buffer_load_dwordx4 v236, s[28:31], s40 offen offset:2048 sc1 lds\n"
    "s_add_u32 m0, s37, 66560\n"
    "v_cvt_pk_bf16_f32 v145, v210, v211\n"
    "v_exp_f32 v212, v212\n"
    "v_mov_b64 v[38:39], 0\n"
    "buffer_load_dwordx4 v237, s[28:31], s40 offen sc1 lds\n"
    "buffer_load_dwordx4 v237, s[28:31], s40 offen offset:2048 sc1 lds\n"
    "s_waitcnt lgkmcnt(3)\n"
    "v_lshlrev_b32 v1, 16, v188\n"
    "v_and_b32 v2, s57, v188\n"
    "v_lshlrev_b32 v3, 16, v189\n"
    "v_and_b32 v238, s57, v189\n"
    "v_mul_f32 v1, s56, v1\n"
    "v_mul_f32 v2, s56, v2\n"
    "v_mul_f32 v3, s56, v3\n"
    "v_mul_f32 v238, s56, v238\n"
    "v_cvt_pk_bf16_f32 v1, v1, v2\n"
    "v_cvt_pk_bf16_f32 v3, v3, v238\n"
    "v_accvgpr_write_b32 a96, v1\n"
    "v_accvgpr_write_b32 a97, v3\n"
    "v_lshlrev_b32 v239, 16, v190\n"
    "v_and_b32 v254, s57, v190\n"
    "v_lshlrev_b32 v255, 16, v191\n"
    "v_and_b32 v253, s57, v191\n"
    "v_mul_f32 v239, s56, v239\n"
    "v_mul_f32 v254, s56, v254\n"
    "v_mul_f32 v255, s56, v255\n"
    "v_mul_f32 v253, s56, v253\n"
    "v_cvt_pk_bf16_f32 v239, v239, v254\n"
    "v_cvt_pk_bf16_f32 v255, v255, v253\n"
    "v_accvgpr_write_b32 a98, v239\n"
    "v_accvgpr_write_b32 a99, v255\n"
    "s_waitcnt lgkmcnt(2)\n"
    "v_lshlrev_b32 v1, 16, v192\n"
    "v_and_b32 v2, s57, v192\n"
    "v_lshlrev_b32 v3, 16, v193\n"
    "v_and_b32 v238, s57, v193\n"
    "v_mul_f32 v1, s56, v1\n"
    "v_mul_f32 v2, s56, v2\n"
    "v_mul_f32 v3, s56, v3\n"
    "v_mul_f32 v238, s56, v238\n"
    "v_cvt_pk_bf16_f32 v1, v1, v2\n"
    "v_cvt_pk_bf16_f32 v3, v3, v238\n"
    "v_accvgpr_write_b32 a100, v1\n"
    "v_accvgpr_write_b32 a101, v3\n"
    "v_lshlrev_b32 v239, 16, v194\n"
    "v_and_b32 v254, s57, v194\n"
    "v_lshlrev_b32 v255, 16, v195\n"
    "v_and_b32 v253, s57, v195\n"
    "v_mul_f32 v239, s56, v239\n"
    "v_mul_f32 v254, s56, v254\n"
    "v_mul_f32 v255, s56, v255\n"
    "v_mul_f32 v253, s56, v253\n"
    "v_cvt_pk_bf16_f32 v239, v239, v254\n"
    "v_cvt_pk_bf16_f32 v255, v255, v253\n"
    "v_accvgpr_write_b32 a102, v239\n"
    "v_accvgpr_write_b32 a103, v255\n"
    "s_waitcnt lgkmcnt(1)\n"
    "v_lshlrev_b32 v1, 16, v196\n"
    "v_and_b32 v2, s57, v196\n"
    "v_lshlrev_b32 v3, 16, v197\n"
    "v_and_b32 v238, s57, v197\n"
    "v_mul_f32 v1, s56, v1\n"
    "v_mul_f32 v2, s56, v2\n"
    "v_mul_f32 v3, s56, v3\n"
    "v_mul_f32 v238, s56, v238\n"
    "v_cvt_pk_bf16_f32 v1, v1, v2\n"
    "v_cvt_pk_bf16_f32 v3, v3, v238\n"
    "v_accvgpr_write_b32 a104, v1\n"
    "v_accvgpr_write_b32 a105, v3\n"
    "v_lshlrev_b32 v239, 16, v198\n"
    "v_and_b32 v254, s57, v198\n"
    "v_lshlrev_b32 v255, 16, v199\n"
    "v_and_b32 v253, s57, v199\n"
    "v_mul_f32 v239, s56, v239\n"
    "v_mul_f32 v254, s56, v254\n"
    "v_mul_f32 v255, s56, v255\n"
    "v_mul_f32 v253, s56, v253\n"
    "v_cvt_pk_bf16_f32 v239, v239, v254\n"
    "v_cvt_pk_bf16_f32 v255, v255, v253\n"
    "v_accvgpr_write_b32 a106, v239\n"
    "v_accvgpr_write_b32 a107, v255\n"
    "s_waitcnt lgkmcnt(0)\n"
    "v_lshlrev_b32 v1, 16, v200\n"
    "v_and_b32 v2, s57, v200\n"
    "v_lshlrev_b32 v3, 16, v201\n"
    "v_and_b32 v238, s57, v201\n"
    "v_mul_f32 v1, s56, v1\n"
    "v_mul_f32 v2, s56, v2\n"
    "v_mul_f32 v3, s56, v3\n"
    "v_mul_f32 v238, s56, v238\n"
    "v_cvt_pk_bf16_f32 v1, v1, v2\n"
    "v_cvt_pk_bf16_f32 v3, v3, v238\n"
    "v_accvgpr_write_b32 a108, v1\n"
    "v_accvgpr_write_b32 a109, v3\n"
    "v_lshlrev_b32 v239, 16, v202\n"
    "v_and_b32 v254, s57, v202\n"
    "v_lshlrev_b32 v255, 16, v203\n"
    "v_and_b32 v253, s57, v203\n"
    "v_mul_f32 v239, s56, v239\n"
    "v_mul_f32 v254, s56, v254\n"
    "v_mul_f32 v255, s56, v255\n"
    "v_mul_f32 v253, s56, v253\n"
    "v_cvt_pk_bf16_f32 v239, v239, v254\n"
    "v_cvt_pk_bf16_f32 v255, v255, v253\n"
    "v_accvgpr_write_b32 a110, v239\n"
    "v_accvgpr_write_b32 a111, v255\n"
    "v_mfma_f32_16x16x32_bf16 v[208:211], a[112:115], a[96:99], 0\n"
    "v_exp_f32 v213, v213\n"
    "v_mfma_f32_16x16x32_bf16 v[208:211], a[116:119], a[100:103], v[208:211]\n"
    "v_exp_f32 v214, v214\n"
    "v_mfma_f32_16x16x32_bf16 v[208:211], a[120:123], a[104:107], v[208:211]\n"
    "v_cvt_pk_bf16_f32 v146, v212, v213\n"
    "v_mov_b64 v[40:41], 0\n"
    "v_mfma_f32_16x16x32_bf16 v[208:211], a[124:127], a[108:111], v[208:211]\n"
    "v_exp_f32 v215, v215\n"
    "s_nop 0\n"
    "v_cvt_pk_bf16_f32 v147, v214, v215\n"
    "v_exp_f32 v216, v216\n"
    "v_mov_b64 v[42:43], 0\n"
    "v_mfma_f32_16x16x32_bf16 v[212:215], a[128:131], a[96:99], 0\n"
    "v_exp_f32 v217, v217\n"
    "v_mfma_f32_16x16x32_bf16 v[212:215], a[132:135], a[100:103], v[212:215]\n"
    "v_exp_f32 v218, v218\n"
    "v_mfma_f32_16x16x32_bf16 v[212:215], a[136:139], a[104:107], v[212:215]\n"
    "v_cvt_pk_bf16_f32 v148, v216, v217\n"
    "v_mov_b64 v[44:45], 0\n"
    "v_mfma_f32_16x16x32_bf16 v[212:215], a[140:143], a[108:111], v[212:215]\n"
    "s_waitcnt vmcnt(8)\n"
    "ds_read_b128 v[72:75], v250 offset:32768\n"
    "ds_read_b128 v[76:79], v251 offset:32768\n"
    "s_add_u32 m0, s37, 16384\n"
    "ds_read_b128 v[80:83], v250 offset:33792\n"
    "ds_read_b128 v[84:87], v251 offset:33792\n"
    "v_exp_f32 v219, v219\n"
    "buffer_load_dwordx4 v236, s[24:27], s41 offen sc1 lds\n"
    "buffer_load_dwordx4 v236, s[24:27], s41 offen offset:2048 sc1 lds\n"
    "s_add_u32 m0, s37, 17408\n"
    "v_cvt_pk_bf16_f32 v149, v218, v219\n"
    "v_exp_f32 v220, v220\n"
    "v_mov_b64 v[46:47], 0\n"
    "buffer_load_dwordx4 v237, s[24:27], s41 offen sc1 lds\n"
    "buffer_load_dwordx4 v237, s[24:27], s41 offen offset:2048 sc1 lds\n"
    "s_waitcnt lgkmcnt(3)\n"
    "v_lshlrev_b32 v1, 16, v72\n"
    "v_and_b32 v2, s57, v72\n"
    "v_lshlrev_b32 v3, 16, v73\n"
    "v_and_b32 v238, s57, v73\n"
    "v_mul_f32 v1, s56, v1\n"
    "v_mul_f32 v2, s56, v2\n"
    "v_mul_f32 v3, s56, v3\n"
    "v_mul_f32 v238, s56, v238\n"
    "v_cvt_pk_bf16_f32 v1, v1, v2\n"
    "v_cvt_pk_bf16_f32 v3, v3, v238\n"
    "v_accvgpr_write_b32 a64, v1\n"
    "v_accvgpr_write_b32 a65, v3\n"
    "v_lshlrev_b32 v239, 16, v74\n"
    "v_and_b32 v254, s57, v74\n"
    "v_lshlrev_b32 v255, 16, v75\n"
    "v_and_b32 v253, s57, v75\n"
    "v_mul_f32 v239, s56, v239\n"
    "v_mul_f32 v254, s56, v254\n"
    "v_mul_f32 v255, s56, v255\n"
    "v_mul_f32 v253, s56, v253\n"
    "v_cvt_pk_bf16_f32 v239, v239, v254\n"
    "v_cvt_pk_bf16_f32 v255, v255, v253\n"
    "v_accvgpr_write_b32 a66, v239\n"
    "v_accvgpr_write_b32 a67, v255\n"
    "s_waitcnt lgkmcnt(2)\n"
    "v_lshlrev_b32 v1, 16, v76\n"
    "v_and_b32 v2, s57, v76\n"
    "v_lshlrev_b32 v3, 16, v77\n"
    "v_and_b32 v238, s57, v77\n"
    "v_mul_f32 v1, s56, v1\n"
    "v_mul_f32 v2, s56, v2\n"
    "v_mul_f32 v3, s56, v3\n"
    "v_mul_f32 v238, s56, v238\n"
    "v_cvt_pk_bf16_f32 v1, v1, v2\n"
    "v_cvt_pk_bf16_f32 v3, v3, v238\n"
    "v_accvgpr_write_b32 a68, v1\n"
    "v_accvgpr_write_b32 a69, v3\n"
    "v_lshlrev_b32 v239, 16, v78\n"
    "v_and_b32 v254, s57, v78\n"
    "v_lshlrev_b32 v255, 16, v79\n"
    "v_and_b32 v253, s57, v79\n"
    "v_mul_f32 v239, s56, v239\n"
    "v_mul_f32 v254, s56, v254\n"
    "v_mul_f32 v255, s56, v255\n"
    "v_mul_f32 v253, s56, v253\n"
    "v_cvt_pk_bf16_f32 v239, v239, v254\n"
    "v_cvt_pk_bf16_f32 v255, v255, v253\n"
    "v_accvgpr_write_b32 a70, v239\n"
    "v_accvgpr_write_b32 a71, v255\n"
    "s_waitcnt lgkmcnt(1)\n"
    "v_lshlrev_b32 v1, 16, v80\n"
    "v_and_b32 v2, s57, v80\n"
    "v_lshlrev_b32 v3, 16, v81\n"
    "v_and_b32 v238, s57, v81\n"
    "v_mul_f32 v1, s56, v1\n"
    "v_mul_f32 v2, s56, v2\n"
    "v_mul_f32 v3, s56, v3\n"
    "v_mul_f32 v238, s56, v238\n"
    "v_cvt_pk_bf16_f32 v1, v1, v2\n"
    "v_cvt_pk_bf16_f32 v3, v3, v238\n"
    "v_accvgpr_write_b32 a72, v1\n"
    "v_accvgpr_write_b32 a73, v3\n"
    "v_lshlrev_b32 v239, 16, v82\n"
    "v_and_b32 v254, s57, v82\n"
    "v_lshlrev_b32 v255, 16, v83\n"
    "v_and_b32 v253, s57, v83\n"
    "v_mul_f32 v239, s56, v239\n"
    "v_mul_f32 v254, s56, v254\n"
    "v_mul_f32 v255, s56, v255\n"
    "v_mul_f32 v253, s56, v253\n"
    "v_cvt_pk_bf16_f32 v239, v239, v254\n"
    "v_cvt_pk_bf16_f32 v255, v255, v253\n"
    "v_accvgpr_write_b32 a74, v239\n"
    "v_accvgpr_write_b32 a75, v255\n"
    "s_waitcnt lgkmcnt(0)\n"
    "v_lshlrev_b32 v1, 16, v84\n"
    "v_and_b32 v2, s57, v84\n"
    "v_lshlrev_b32 v3, 16, v85\n"
    "v_and_b32 v238, s57, v85\n"
    "v_mul_f32 v1, s56, v1\n"
    "v_mul_f32 v2, s56, v2\n"
    "v_mul_f32 v3, s56, v3\n"
    "v_mul_f32 v238, s56, v238\n"
    "v_cvt_pk_bf16_f32 v1, v1, v2\n"
    "v_cvt_pk_bf16_f32 v3, v3, v238\n"
    "v_accvgpr_write_b32 a76, v1\n"
    "v_accvgpr_write_b32 a77, v3\n"
    "v_lshlrev_b32 v239, 16, v86\n"
    "v_and_b32 v254, s57, v86\n"
    "v_lshlrev_b32 v255, 16, v87\n"
    "v_and_b32 v253, s57, v87\n"
    "v_mul_f32 v239, s56, v239\n"
    "v_mul_f32 v254, s56, v254\n"
    "v_mul_f32 v255, s56, v255\n"
    "v_mul_f32 v253, s56, v253\n"
    "v_cvt_pk_bf16_f32 v239, v239, v254\n"
    "v_cvt_pk_bf16_f32 v255, v255, v253\n"
    "v_accvgpr_write_b32 a78, v239\n"
    "v_accvgpr_write_b32 a79, v255\n"
    "v_mfma_f32_16x16x32_bf16 v[216:219], a[112:115], a[64:67], 0\n"
    "v_exp_f32 v221, v221\n"
    "v_mfma_f32_16x16x32_bf16 v[216:219], a[116:119], a[68:71], v[216:219]\n"
    "v_exp_f32 v222, v222\n"
    "v_mfma_f32_16x16x32_bf16 v[216:219], a[120:123], a[72:75], v[216:219]\n"
    "v_cvt_pk_bf16_f32 v150, v220, v221\n"
    "v_mov_b64 v[48:49], 0\n"
    "v_mfma_f32_16x16x32_bf16 v[216:219], a[124:127], a[76:79], v[216:219]\n"
    "v_exp_f32 v223, v223\n"
    "s_nop 0\n"
    "v_cvt_pk_bf16_f32 v151, v222, v223\n"
    "v_exp_f32 v208, v208\n"
    "v_mov_b64 v[50:51], 0\n"
    "v_mfma_f32_16x16x32_bf16 v[220:223], a[128:131], a[64:67], 0\n"
    "v_exp_f32 v209, v209\n"
    "v_mfma_f32_16x16x32_bf16 v[220:223], a[132:135], a[68:71], v[220:223]\n"
    "v_exp_f32 v210, v210\n"
    "v_mfma_f32_16x16x32_bf16 v[220:223], a[136:139], a[72:75], v[220:223]\n"
    "v_cvt_pk_bf16_f32 v168, v208, v209\n"
    "v_mov_b64 v[52:53], 0\n"
    "v_mfma_f32_16x16x32_bf16 v[220:223], a[140:143], a[76:79], v[220:223]\n"
    "s_waitcnt vmcnt(8)\n"
    "ds_read_b128 v[104:107], v250 offset:49152\n"
    "ds_read_b128 v[108:111], v251 offset:49152\n"
    "s_add_u32 m0, s37, 81920\n"
    "ds_read_b128 v[112:115], v250 offset:50176\n"
    "ds_read_b128 v[116:119], v251 offset:50176\n"
    "v_exp_f32 v211, v211\n"
    "buffer_load_dwordx4 v236, s[28:31], s41 offen sc1 lds\n"
    "buffer_load_dwordx4 v236, s[28:31], s41 offen offset:2048 sc1 lds\n"
    "s_add_u32 m0, s37, 82944\n"
    "v_cvt_pk_bf16_f32 v169, v210, v211\n"
    "v_exp_f32 v212, v212\n"
    "v_mov_b64 v[54:55], 0\n"
    "buffer_load_dwordx4 v237, s[28:31], s41 offen sc1 lds\n"
    "buffer_load_dwordx4 v237, s[28:31], s41 offen offset:2048 sc1 lds\n"
    "s_waitcnt lgkmcnt(3)\n"
    "v_lshlrev_b32 v1, 16, v104\n"
    "v_and_b32 v2, s57, v104\n"
    "v_lshlrev_b32 v3, 16, v105\n"
    "v_and_b32 v238, s57, v105\n"
    "v_mul_f32 v1, s56, v1\n"
    "v_mul_f32 v2, s56, v2\n"
    "v_mul_f32 v3, s56, v3\n"
    "v_mul_f32 v238, s56, v238\n"
    "v_cvt_pk_bf16_f32 v1, v1, v2\n"
    "v_cvt_pk_bf16_f32 v3, v3, v238\n"
    "v_accvgpr_write_b32 a80, v1\n"
    "v_accvgpr_write_b32 a81, v3\n"
    "v_lshlrev_b32 v239, 16, v106\n"
    "v_and_b32 v254, s57, v106\n"
    "v_lshlrev_b32 v255, 16, v107\n"
    "v_and_b32 v253, s57, v107\n"
    "v_mul_f32 v239, s56, v239\n"
    "v_mul_f32 v254, s56, v254\n"
    "v_mul_f32 v255, s56, v255\n"
    "v_mul_f32 v253, s56, v253\n"
    "v_cvt_pk_bf16_f32 v239, v239, v254\n"
    "v_cvt_pk_bf16_f32 v255, v255, v253\n"
    "v_accvgpr_write_b32 a82, v239\n"
    "v_accvgpr_write_b32 a83, v255\n"
    "s_waitcnt lgkmcnt(2)\n"
    "v_lshlrev_b32 v1, 16, v108\n"
    "v_and_b32 v2, s57, v108\n"
    "v_lshlrev_b32 v3, 16, v109\n"
    "v_and_b32 v238, s57, v109\n"
    "v_mul_f32 v1, s56, v1\n"
    "v_mul_f32 v2, s56, v2\n"
    "v_mul_f32 v3, s56, v3\n"
    "v_mul_f32 v238, s56, v238\n"
    "v_cvt_pk_bf16_f32 v1, v1, v2\n"
    "v_cvt_pk_bf16_f32 v3, v3, v238\n"
    "v_accvgpr_write_b32 a84, v1\n"
    "v_accvgpr_write_b32 a85, v3\n"
    "v_lshlrev_b32 v239, 16, v110\n"
    "v_and_b32 v254, s57, v110\n"
    "v_lshlrev_b32 v255, 16, v111\n"
    "v_and_b32 v253, s57, v111\n"
    "v_mul_f32 v239, s56, v239\n"
    "v_mul_f32 v254, s56, v254\n"
    "v_mul_f32 v255, s56, v255\n"
    "v_mul_f32 v253, s56, v253\n"
    "v_cvt_pk_bf16_f32 v239, v239, v254\n"
    "v_cvt_pk_bf16_f32 v255, v255, v253\n"
    "v_accvgpr_write_b32 a86, v239\n"
    "v_accvgpr_write_b32 a87, v255\n"
    "s_waitcnt lgkmcnt(1)\n"
    "v_lshlrev_b32 v1, 16, v112\n"
    "v_and_b32 v2, s57, v112\n"
    "v_lshlrev_b32 v3, 16, v113\n"
    "v_and_b32 v238, s57, v113\n"
    "v_mul_f32 v1, s56, v1\n"
    "v_mul_f32 v2, s56, v2\n"
    "v_mul_f32 v3, s56, v3\n"
    "v_mul_f32 v238, s56, v238\n"
    "v_cvt_pk_bf16_f32 v1, v1, v2\n"
    "v_cvt_pk_bf16_f32 v3, v3, v238\n"
    "v_accvgpr_write_b32 a88, v1\n"
    "v_accvgpr_write_b32 a89, v3\n"
    "v_lshlrev_b32 v239, 16, v114\n"
    "v_and_b32 v254, s57, v114\n"
    "v_lshlrev_b32 v255, 16, v115\n"
    "v_and_b32 v253, s57, v115\n"
    "v_mul_f32 v239, s56, v239\n"
    "v_mul_f32 v254, s56, v254\n"
    "v_mul_f32 v255, s56, v255\n"
    "v_mul_f32 v253, s56, v253\n"
    "v_cvt_pk_bf16_f32 v239, v239, v254\n"
    "v_cvt_pk_bf16_f32 v255, v255, v253\n"
    "v_accvgpr_write_b32 a90, v239\n"
    "v_accvgpr_write_b32 a91, v255\n"
    "s_waitcnt lgkmcnt(0)\n"
    "v_lshlrev_b32 v1, 16, v116\n"
    "v_and_b32 v2, s57, v116\n"
    "v_lshlrev_b32 v3, 16, v117\n"
    "v_and_b32 v238, s57, v117\n"
    "v_mul_f32 v1, s56, v1\n"
    "v_mul_f32 v2, s56, v2\n"
    "v_mul_f32 v3, s56, v3\n"
    "v_mul_f32 v238, s56, v238\n"
    "v_cvt_pk_bf16_f32 v1, v1, v2\n"
    "v_cvt_pk_bf16_f32 v3, v3, v238\n"
    "v_accvgpr_write_b32 a92, v1\n"
    "v_accvgpr_write_b32 a93, v3\n"
    "v_lshlrev_b32 v239, 16, v118\n"
    "v_and_b32 v254, s57, v118\n"
    "v_lshlrev_b32 v255, 16, v119\n"
    "v_and_b32 v253, s57, v119\n"
    "v_mul_f32 v239, s56, v239\n"
    "v_mul_f32 v254, s56, v254\n"
    "v_mul_f32 v255, s56, v255\n"
    "v_mul_f32 v253, s56, v253\n"
    "v_cvt_pk_bf16_f32 v239, v239, v254\n"
    "v_cvt_pk_bf16_f32 v255, v255, v253\n"
    "v_accvgpr_write_b32 a94, v239\n"
    "v_accvgpr_write_b32 a95, v255\n"
    "v_mfma_f32_16x16x32_bf16 v[208:211], a[112:115], a[80:83], 0\n"
    "v_exp_f32 v213, v213\n"
    "v_mfma_f32_16x16x32_bf16 v[208:211], a[116:119], a[84:87], v[208:211]\n"
    "v_exp_f32 v214, v214\n"
    "v_mfma_f32_16x16x32_bf16 v[208:211], a[120:123], a[88:91], v[208:211]\n"
    "v_cvt_pk_bf16_f32 v170, v212, v213\n"
    "v_mov_b64 v[56:57], 0\n"
    "v_mfma_f32_16x16x32_bf16 v[208:211], a[124:127], a[92:95], v[208:211]\n"
    "v_exp_f32 v215, v215\n"
    "s_nop 0\n"
    "v_cvt_pk_bf16_f32 v171, v214, v215\n"
    "v_exp_f32 v216, v216\n"
    "v_mov_b64 v[58:59], 0\n"
    "v_mfma_f32_16x16x32_bf16 v[212:215], a[128:131], a[80:83], 0\n"
    "v_exp_f32 v217, v217\n"
    "v_mfma_f32_16x16x32_bf16 v[212:215], a[132:135], a[84:87], v[212:215]\n"
    "v_exp_f32 v218, v218\n"
    "v_mfma_f32_16x16x32_bf16 v[212:215], a[136:139], a[88:91], v[212:215]\n"
    "v_cvt_pk_bf16_f32 v152, v216, v217\n"
    "v_mov_b64 v[60:61], 0\n"
    "v_mfma_f32_16x16x32_bf16 v[212:215], a[140:143], a[92:95], v[212:215]\n"
    "v_exp_f32 v219, v219\n"
    "v_mov_b64 v[62:63], 0\n"
    "v_mov_b64 v[64:65], 0\n"
    "v_mov_b64 v[66:67], 0\n"
    "v_mov_b64 v[68:69], 0\n"
    "v_mov_b64 v[70:71], 0\n"
    "v_mov_b64 v[72:73], 0\n"
    "v_mov_b64 v[74:75], 0\n"
    "v_mov_b64 v[76:77], 0\n"
    "v_mov_b64 v[78:79], 0\n"
    "v_mov_b64 v[80:81], 0\n"
    "v_mov_b64 v[82:83], 0\n"
    "v_mov_b64 v[84:85], 0\n"
    "v_mov_b64 v[86:87], 0\n"
    "v_mov_b64 v[88:89], 0\n"
    "v_mov_b64 v[90:91], 0\n"
    "v_mov_b64 v[92:93], 0\n"
    "v_mov_b64 v[94:95], 0\n"
    "v_mov_b64 v[96:97], 0\n"
    "v_mov_b64 v[98:99], 0\n"
    "v_mov_b64 v[100:101], 0\n"
    "v_mov_b64 v[102:103], 0\n"
    "v_mov_b64 v[104:105], 0\n"
    "v_mov_b64 v[106:107], 0\n"
    "v_mov_b64 v[108:109], 0\n"
    "v_mov_b64 v[110:111], 0\n"
    "v_mov_b64 v[112:113], 0\n"
    "v_mov_b64 v[114:115], 0\n"
    "v_mov_b64 v[116:117], 0\n"
    "v_mov_b64 v[118:119], 0\n"
    "v_mov_b64 v[120:121], 0\n"
    "v_mov_b64 v[122:123], 0\n"
    "v_mov_b64 v[124:125], 0\n"
    "v_mov_b64 v[126:127], 0\n"
    "v_mov_b64 v[128:129], 0\n"
    "v_mov_b64 v[130:131], 0\n"
    "v_mov_b64 v[132:133], 0\n"
    "v_mov_b64 v[134:135], 0\n"
    "v_cvt_pk_bf16_f32 v153, v218, v219\n"
    "v_exp_f32 v220, v220\n"
    "v_mfma_f32_16x16x32_bf16 v[216:219], a[144:147], a[64:67], 0\n"
    "v_exp_f32 v221, v221\n"
    "v_mfma_f32_16x16x32_bf16 v[216:219], a[148:151], a[68:71], v[216:219]\n"
    "v_exp_f32 v222, v222\n"
    "v_mfma_f32_16x16x32_bf16 v[216:219], a[152:155], a[72:75], v[216:219]\n"
    "v_cvt_pk_bf16_f32 v154, v220, v221\n"
    "v_mfma_f32_16x16x32_bf16 v[216:219], a[156:159], a[76:79], v[216:219]\n"
    "v_exp_f32 v223, v223\n"
    "s_nop 0\n"
    "v_cvt_pk_bf16_f32 v155, v222, v223\n"
    "v_exp_f32 v208, v208\n"
    "v_mfma_f32_16x16x32_bf16 v[220:223], a[160:163], a[64:67], 0\n"
    "v_exp_f32 v209, v209\n"
    "v_mfma_f32_16x16x32_bf16 v[220:223], a[164:167], a[68:71], v[220:223]\n"
    "v_exp_f32 v210, v210\n"
    "v_mfma_f32_16x16x32_bf16 v[220:223], a[168:171], a[72:75], v[220:223]\n"
    "v_cvt_pk_bf16_f32 v160, v208, v209\n"
    "v_mfma_f32_16x16x32_bf16 v[220:223], a[172:175], a[76:79], v[220:223]\n"
    "v_exp_f32 v211, v211\n"
    "s_nop 0\n"
    "v_cvt_pk_bf16_f32 v161, v210, v211\n"
    "v_exp_f32 v212, v212\n"
    "v_mfma_f32_16x16x32_bf16 v[208:211], a[144:147], a[80:83], 0\n"
    "v_exp_f32 v213, v213\n"
    "v_mfma_f32_16x16x32_bf16 v[208:211], a[148:151], a[84:87], v[208:211]\n"
    "v_exp_f32 v214, v214\n"
    "v_mfma_f32_16x16x32_bf16 v[208:211], a[152:155], a[88:91], v[208:211]\n"
    "v_cvt_pk_bf16_f32 v162, v212, v213\n"
    "v_mfma_f32_16x16x32_bf16 v[208:211], a[156:159], a[92:95], v[208:211]\n"
    "v_exp_f32 v215, v215\n"
    "s_nop 0\n"
    "v_cvt_pk_bf16_f32 v163, v214, v215\n"
    "v_exp_f32 v216, v216\n"
    "v_mfma_f32_16x16x32_bf16 v[212:215], a[160:163], a[80:83], 0\n"
    "v_exp_f32 v217, v217\n"
    "v_mfma_f32_16x16x32_bf16 v[212:215], a[164:167], a[84:87], v[212:215]\n"
    "v_exp_f32 v218, v218\n"
    "v_mfma_f32_16x16x32_bf16 v[212:215], a[168:171], a[88:91], v[212:215]\n"
    "v_cvt_pk_bf16_f32 v156, v216, v217\n"
    "v_mfma_f32_16x16x32_bf16 v[212:215], a[172:175], a[92:95], v[212:215]\n"
    "s_waitcnt vmcnt(8)\n"
    "s_barrier\n"
    "s_add_u32 m0, s37, 32768\n"
    "ds_read_b64_tr_b16 a[176:177], v228 offset:0\n"
    "ds_read_b64_tr_b16 a[178:179], v228 offset:4096\n"
    "buffer_load_dwordx4 v236, s[24:27], s42 offen sc1 lds\n"
    "buffer_load_dwordx4 v236, s[24:27], s42 offen offset:2048 sc1 lds\n"
    "s_add_u32 m0, s37, 33792\n"
    "ds_read_b64_tr_b16 a[180:181], v230 offset:0\n"
    "ds_read_b64_tr_b16 a[182:183], v230 offset:4096\n"
    "ds_read_b64_tr_b16 a[184:185], v228 offset:8\n"
    "ds_read_b64_tr_b16 a[186:187], v228 offset:4104\n"
    "buffer_load_dwordx4 v237, s[24:27], s42 offen sc1 lds\n"
    "buffer_load_dwordx4 v237, s[24:27], s42 offen offset:2048 sc1 lds\n"
    "ds_read_b64_tr_b16 a[188:189], v230 offset:8\n"
    "ds_read_b64_tr_b16 a[190:191], v230 offset:4104\n"
    "v_exp_f32 v219, v219\n"
    "ds_read_b64_tr_b16 a[192:193], v229 offset:0\n"
    "v_cvt_pk_bf16_f32 v157, v218, v219\n"
    "v_exp_f32 v220, v220\n"
    "ds_read_b64_tr_b16 a[194:195], v229 offset:4096\n"
    "ds_read_b64_tr_b16 a[196:197], v231 offset:0\n"
    "v_exp_f32 v221, v221\n"
    "s_waitcnt lgkmcnt(9)\n"
    "v_mfma_f32_16x16x32_bf16 v[8:11], a[176:179], v[136:139], v[8:11]\n"
    "s_waitcnt lgkmcnt(3)\n"
    "v_exp_f32 v222, v222\n"
    "ds_read_b64_tr_b16 a[198:199], v231 offset:4096\n"
    "v_cvt_pk_bf16_f32 v158, v220, v221\n"
    "v_exp_f32 v223, v223\n"
    "v_mfma_f32_16x16x32_bf16 v[8:11], a[180:183], v[140:143], v[8:11]\n"
    "v_exp_f32 v208, v208\n"
    "v_mfma_f32_16x16x32_bf16 v[12:15], a[184:187], v[136:139], v[12:15]\n"
    "ds_read_b64_tr_b16 a[200:201], v229 offset:8\n"
    "v_cvt_pk_bf16_f32 v159, v222, v223\n"
    "v_mfma_f32_16x16x32_bf16 v[12:15], a[188:191], v[140:143], v[12:15]\n"
    "v_exp_f32 v209, v209\n"
    "v_mfma_f32_16x16x32_bf16 a[208:211], a[228:231], v[136:139], a[208:211]\n"
    "v_exp_f32 v210, v210\n"
    "v_mfma_f32_16x16x32_bf16 a[208:211], a[228:231], v[140:143], a[208:211]\n"
    "ds_read_b64_tr_b16 a[202:203], v229 offset:4104\n"
    "v_cvt_pk_bf16_f32 v164, v208, v209\n"
    "v_mfma_f32_16x16x32_bf16 a[0:3], a[176:179], v[168:171], a[0:3]\n"
    "v_exp_f32 v211, v211\n"
    "v_mfma_f32_16x16x32_bf16 v[40:43], a[176:179], v[144:147], v[40:43]\n"
    "ds_read_b64_tr_b16 a[204:205], v231 offset:8\n"
    "v_cvt_pk_bf16_f32 v165, v210, v211\n"
    "v_mfma_f32_16x16x32_bf16 v[40:43], a[180:183], v[148:151], v[40:43]\n"
    "v_exp_f32 v212, v212\n"
    "v_mfma_f32_16x16x32_bf16 v[44:47], a[184:187], v[144:147], v[44:47]\n"
    "v_exp_f32 v213, v213\n"
    "v_mfma_f32_16x16x32_bf16 v[44:47], a[188:191], v[148:151], v[44:47]\n"
    "v_exp_f32 v214, v214\n"
    "v_mfma_f32_16x16x32_bf16 a[212:215], a[228:231], v[144:147], a[212:215]\n"
    "ds_read_b64_tr_b16 a[206:207], v231 offset:4104\n"
    "v_cvt_pk_bf16_f32 v166, v212, v213\n"
    "v_mfma_f32_16x16x32_bf16 a[212:215], a[228:231], v[148:151], a[212:215]\n"
    "v_exp_f32 v215, v215\n"
    "v_mfma_f32_16x16x32_bf16 v[72:75], a[176:179], v[152:155], v[72:75]\n"
    "v_cvt_pk_bf16_f32 v167, v214, v215\n"
    "v_mfma_f32_16x16x32_bf16 v[72:75], a[180:183], v[156:159], v[72:75]\n"
    "v_mfma_f32_16x16x32_bf16 a[4:7], a[184:187], v[168:171], a[4:7]\n"
    "v_mfma_f32_16x16x32_bf16 v[76:79], a[184:187], v[152:155], v[76:79]\n"
    "v_mfma_f32_16x16x32_bf16 v[76:79], a[188:191], v[156:159], v[76:79]\n"
    "v_mfma_f32_16x16x32_bf16 a[216:219], a[228:231], v[152:155], a[216:219]\n"
    "v_mfma_f32_16x16x32_bf16 a[216:219], a[228:231], v[156:159], a[216:219]\n"
    "v_mfma_f32_16x16x32_bf16 v[104:107], a[176:179], v[160:163], v[104:107]\n"
    "v_mfma_f32_16x16x32_bf16 v[104:107], a[180:183], v[164:167], v[104:107]\n"
    "ds_read_b64_tr_b16 a[176:177], v228 offset:1024\n"
    "ds_read_b64_tr_b16 a[178:179], v228 offset:5120\n"
    "v_mfma_f32_16x16x32_bf16 v[108:111], a[184:187], v[160:163], v[108:111]\n"
    "ds_read_b64_tr_b16 a[180:181], v230 offset:1024\n"
    "ds_read_b64_tr_b16 a[182:183], v230 offset:5120\n"
    "v_mfma_f32_16x16x32_bf16 v[108:111], a[188:191], v[164:167], v[108:111]\n"
    "ds_read_b64_tr_b16 a[184:185], v228 offset:1032\n"
    "ds_read_b64_tr_b16 a[186:187], v228 offset:5128\n"
    "v_mfma_f32_16x16x32_bf16 a[224:227], a[228:231], v[168:171], a[224:227]\n"
    "ds_read_b64_tr_b16 a[188:189], v230 offset:1032\n"
    "ds_read_b64_tr_b16 a[190:191], v230 offset:5128\n"
    "v_mfma_f32_16x16x32_bf16 a[220:223], a[228:231], v[160:163], a[220:223]\n"
    "v_mfma_f32_16x16x32_bf16 a[220:223], a[228:231], v[164:167], a[220:223]\n"
    "s_waitcnt lgkmcnt(4)\n"
    "v_mfma_f32_16x16x32_bf16 v[16:19], a[192:195], v[136:139], v[16:19]\n"
    "v_mfma_f32_16x16x32_bf16 v[16:19], a[196:199], v[140:143], v[16:19]\n"
    "v_mfma_f32_16x16x32_bf16 v[20:23], a[200:203], v[136:139], v[20:23]\n"
    "v_mfma_f32_16x16x32_bf16 v[20:23], a[204:207], v[140:143], v[20:23]\n"
    "v_mfma_f32_16x16x32_bf16 v[48:51], a[192:195], v[144:147], v[48:51]\n"
    "v_mfma_f32_16x16x32_bf16 v[48:51], a[196:199], v[148:151], v[48:51]\n"
    "v_mfma_f32_16x16x32_bf16 a[8:11], a[192:195], v[168:171], a[8:11]\n"
    "v_mfma_f32_16x16x32_bf16 v[52:55], a[200:203], v[144:147], v[52:55]\n"
    "v_mfma_f32_16x16x32_bf16 v[52:55], a[204:207], v[148:151], v[52:55]\n"
    "v_mfma_f32_16x16x32_bf16 v[80:83], a[192:195], v[152:155], v[80:83]\n"
    "v_mfma_f32_16x16x32_bf16 v[80:83], a[196:199], v[156:159], v[80:83]\n"
    "v_mfma_f32_16x16x32_bf16 v[84:87], a[200:203], v[152:155], v[84:87]\n"
    "v_mfma_f32_16x16x32_bf16 v[84:87], a[204:207], v[156:159], v[84:87]\n"
    "v_mfma_f32_16x16x32_bf16 v[112:115], a[192:195], v[160:163], v[112:115]\n"
    "v_mfma_f32_16x16x32_bf16 v[112:115], a[196:199], v[164:167], v[112:115]\n"
    "ds_read_b64_tr_b16 a[192:193], v229 offset:1024\n"
    "ds_read_b64_tr_b16 a[194:195], v229 offset:5120\n"
    "v_mfma_f32_16x16x32_bf16 a[12:15], a[200:203], v[168:171], a[12:15]\n"
    "ds_read_b64_tr_b16 a[196:197], v231 offset:1024\n"
    "ds_read_b64_tr_b16 a[198:199], v231 offset:5120\n"
    "v_mfma_f32_16x16x32_bf16 v[116:119], a[200:203], v[160:163], v[116:119]\n"
    "v_mfma_f32_16x16x32_bf16 v[116:119], a[204:207], v[164:167], v[116:119]\n"
    "ds_read_b64_tr_b16 a[200:201], v229 offset:1032\n"
    "ds_read_b64_tr_b16 a[202:203], v229 offset:5128\n"
    "v_mfma_f32_16x16x32_bf16 v[24:27], a[176:179], v[136:139], v[24:27]\n"
    "ds_read_b64_tr_b16 a[204:205], v231 offset:1032\n"
    "ds_read_b64_tr_b16 a[206:207], v231 offset:5128\n"
    "v_mfma_f32_16x16x32_bf16 v[24:27], a[180:183], v[140:143], v[24:27]\n"
    "s_waitcnt lgkmcnt(4)\n"
    "v_mfma_f32_16x16x32_bf16 v[28:31], a[184:187], v[136:139], v[28:31]\n"
    "v_mfma_f32_16x16x32_bf16 v[28:31], a[188:191], v[140:143], v[28:31]\n"
    "v_mfma_f32_16x16x32_bf16 v[56:59], a[176:179], v[144:147], v[56:59]\n"
    "v_mfma_f32_16x16x32_bf16 v[56:59], a[180:183], v[148:151], v[56:59]\n"
    "v_mfma_f32_16x16x32_bf16 a[16:19], a[176:179], v[168:171], a[16:19]\n"
    "v_mfma_f32_16x16x32_bf16 v[60:63], a[184:187], v[144:147], v[60:63]\n"
    "v_mfma_f32_16x16x32_bf16 v[60:63], a[188:191], v[148:151], v[60:63]\n"
    "v_mfma_f32_16x16x32_bf16 v[88:91], a[176:179], v[152:155], v[88:91]\n"
    "v_mfma_f32_16x16x32_bf16 v[88:91], a[180:183], v[156:159], v[88:91]\n"
    "v_mfma_f32_16x16x32_bf16 v[92:95], a[184:187], v[152:155], v[92:95]\n"
    "v_mfma_f32_16x16x32_bf16 v[92:95], a[188:191], v[156:159], v[92:95]\n"
    "v_mfma_f32_16x16x32_bf16 v[120:123], a[176:179], v[160:163], v[120:123]\n"
    "v_mfma_f32_16x16x32_bf16 v[120:123], a[180:183], v[164:167], v[120:123]\n"
    "v_mfma_f32_16x16x32_bf16 a[20:23], a[184:187], v[168:171], a[20:23]\n"
    "v_mfma_f32_16x16x32_bf16 v[124:127], a[184:187], v[160:163], v[124:127]\n"
    "v_mfma_f32_16x16x32_bf16 v[124:127], a[188:191], v[164:167], v[124:127]\n"
    "v_mfma_f32_16x16x32_bf16 v[32:35], a[192:195], v[136:139], v[32:35]\n"
    "v_mfma_f32_16x16x32_bf16 v[32:35], a[196:199], v[140:143], v[32:35]\n"
    "s_waitcnt lgkmcnt(0)\n"
    "v_mfma_f32_16x16x32_bf16 v[36:39], a[200:203], v[136:139], v[36:39]\n"
    "v_mfma_f32_16x16x32_bf16 v[36:39], a[204:207], v[140:143], v[36:39]\n"
    "v_mfma_f32_16x16x32_bf16 v[64:67], a[192:195], v[144:147], v[64:67]\n"
    "v_mfma_f32_16x16x32_bf16 v[64:67], a[196:199], v[148:151], v[64:67]\n"
    "v_mfma_f32_16x16x32_bf16 a[24:27], a[192:195], v[168:171], a[24:27]\n"
    "v_mfma_f32_16x16x32_bf16 v[68:71], a[200:203], v[144:147], v[68:71]\n"
    "v_mfma_f32_16x16x32_bf16 v[68:71], a[204:207], v[148:151], v[68:71]\n"
    "v_mfma_f32_16x16x32_bf16 v[96:99], a[192:195], v[152:155], v[96:99]\n"
    "v_mfma_f32_16x16x32_bf16 v[96:99], a[196:199], v[156:159], v[96:99]\n"
    "v_mfma_f32_16x16x32_bf16 v[100:103], a[200:203], v[152:155], v[100:103]\n"
    "v_mfma_f32_16x16x32_bf16 v[100:103], a[204:207], v[156:159], v[100:103]\n"
    "v_mfma_f32_16x16x32_bf16 v[128:131], a[192:195], v[160:163], v[128:131]\n"
    "v_mfma_f32_16x16x32_bf16 v[128:131], a[196:199], v[164:167], v[128:131]\n"
    "v_mfma_f32_16x16x32_bf16 a[28:31], a[200:203], v[168:171], a[28:31]\n"
    "v_mfma_f32_16x16x32_bf16 v[132:135], a[200:203], v[160:163], v[132:135]\n"
    "v_mfma_f32_16x16x32_bf16 v[132:135], a[204:207], v[164:167], v[132:135]\n"
    "s_waitcnt vmcnt(8)\n"
    "s_barrier\n"
    "s_add_u32 m0, s37, 98304\n"
    "ds_read_b128 a[112:115], v224 offset:16384\n"
    "ds_read_b128 a[116:119], v225 offset:16384\n"
    "buffer_load_dwordx4 v236, s[28:31], s42 offen sc1 lds\n"
    "buffer_load_dwordx4 v236, s[28:31], s42 offen offset:2048 sc1 lds\n"
    "s_add_u32 m0, s37, 99328\n"
    "ds_read_b128 a[120:123], v224 offset:17408\n"
    "ds_read_b128 a[124:127], v225 offset:17408\n"
    "ds_read_b128 a[128:131], v224 offset:20480\n"
    "ds_read_b128 a[132:135], v225 offset:20480\n"
    "buffer_load_dwordx4 v237, s[28:31], s42 offen sc1 lds\n"
    "buffer_load_dwordx4 v237, s[28:31], s42 offen offset:2048 sc1 lds\n"
    "s_add_u32 m0, s37, 49152\n"
    "ds_read_b128 a[136:139], v224 offset:21504\n"
    "ds_read_b128 a[140:143], v225 offset:21504\n"
    "ds_read_b128 a[144:147], v226 offset:16384\n"
    "ds_read_b128 a[148:151], v227 offset:16384\n"
    "buffer_load_dwordx4 v236, s[24:27], s43 offen sc1 lds\n"
    "buffer_load_dwordx4 v236, s[24:27], s43 offen offset:2048 sc1 lds\n"
    "s_add_u32 m0, s37, 50176\n"
    "ds_read_b128 a[152:155], v226 offset:17408\n"
    "ds_read_b128 a[156:159], v227 offset:17408\n"
    "ds_read_b128 a[160:163], v226 offset:20480\n"
    "ds_read_b128 a[164:167], v227 offset:20480\n"
    "s_waitcnt lgkmcnt(13)\n"
    "v_mfma_f32_16x16x32_bf16 v[216:219], a[112:115], a[32:35], 0\n"
    "s_waitcnt lgkmcnt(8)\n"
    "buffer_load_dwordx4 v237, s[24:27], s43 offen sc1 lds\n"
    "v_mfma_f32_16x16x32_bf16 v[216:219], a[116:119], a[36:39], v[216:219]\n"
    "buffer_load_dwordx4 v237, s[24:27], s43 offen offset:2048 sc1 lds\n"
    "v_mfma_f32_16x16x32_bf16 v[216:219], a[120:123], a[40:43], v[216:219]\n"
    "ds_read_b128 a[168:171], v226 offset:21504\n"
    "ds_read_b128 a[172:175], v227 offset:21504\n"
    "v_mfma_f32_16x16x32_bf16 v[216:219], a[124:127], a[44:47], v[216:219]\n"
    "v_mfma_f32_16x16x32_bf16 v[220:223], a[128:131], a[32:35], 0\n"
    "v_mfma_f32_16x16x32_bf16 v[220:223], a[132:135], a[36:39], v[220:223]\n"
    "s_waitcnt lgkmcnt(2)\n"
    "v_mfma_f32_16x16x32_bf16 v[220:223], a[136:139], a[40:43], v[220:223]\n"
    "v_mfma_f32_16x16x32_bf16 v[220:223], a[140:143], a[44:47], v[220:223]\n"
    "v_exp_f32 v216, v216\n"
    "v_mfma_f32_16x16x32_bf16 v[208:211], a[112:115], a[48:51], 0\n"
    "v_exp_f32 v217, v217\n"
    "v_mfma_f32_16x16x32_bf16 v[208:211], a[116:119], a[52:55], v[208:211]\n"
    "v_exp_f32 v218, v218\n"
    "v_mfma_f32_16x16x32_bf16 v[208:211], a[120:123], a[56:59], v[208:211]\n"
    "v_cvt_pk_bf16_f32 v172, v216, v217\n"
    "v_mfma_f32_16x16x32_bf16 v[208:211], a[124:127], a[60:63], v[208:211]\n"
    "v_exp_f32 v219, v219\n"
    "v_mfma_f32_16x16x32_bf16 v[212:215], a[128:131], a[48:51], 0\n"
    "v_cvt_pk_bf16_f32 v173, v218, v219\n"
    "v_mfma_f32_16x16x32_bf16 v[212:215], a[132:135], a[52:55], v[212:215]\n"
    "v_exp_f32 v220, v220\n"
    "v_mfma_f32_16x16x32_bf16 v[212:215], a[136:139], a[56:59], v[212:215]\n"
    "v_exp_f32 v221, v221\n"
    "v_mfma_f32_16x16x32_bf16 v[212:215], a[140:143], a[60:63], v[212:215]\n"
    "v_exp_f32 v222, v222\n"
    "v_mfma_f32_16x16x32_bf16 v[216:219], a[112:115], a[64:67], 0\n"
    "v_cvt_pk_bf16_f32 v174, v220, v221\n"
    "v_mfma_f32_16x16x32_bf16 v[216:219], a[116:119], a[68:71], v[216:219]\n"
    "v_exp_f32 v223, v223\n"
    "v_mfma_f32_16x16x32_bf16 v[216:219], a[120:123], a[72:75], v[216:219]\n"
    "v_cvt_pk_bf16_f32 v175, v222, v223\n"
    "v_mfma_f32_16x16x32_bf16 v[216:219], a[124:127], a[76:79], v[216:219]\n"
    "v_exp_f32 v208, v208\n"
    "v_mfma_f32_16x16x32_bf16 v[220:223], a[128:131], a[64:67], 0\n"
    "v_exp_f32 v209, v209\n"
    "v_mfma_f32_16x16x32_bf16 v[220:223], a[132:135], a[68:71], v[220:223]\n"
    "v_exp_f32 v210, v210\n"
    "v_mfma_f32_16x16x32_bf16 v[220:223], a[136:139], a[72:75], v[220:223]\n"
    "v_cvt_pk_bf16_f32 v180, v208, v209\n"
    "v_mfma_f32_16x16x32_bf16 v[220:223], a[140:143], a[76:79], v[220:223]\n"
    "v_exp_f32 v211, v211\n"
    "s_nop 0\n"
    "v_cvt_pk_bf16_f32 v181, v210, v211\n"
    "v_exp_f32 v212, v212\n"
    "v_mfma_f32_16x16x32_bf16 v[208:211], a[112:115], a[80:83], 0\n"
    "v_exp_f32 v213, v213\n"
    "v_mfma_f32_16x16x32_bf16 v[208:211], a[116:119], a[84:87], v[208:211]\n"
    "v_exp_f32 v214, v214\n"
    "v_mfma_f32_16x16x32_bf16 v[208:211], a[120:123], a[88:91], v[208:211]\n"
    "v_cvt_pk_bf16_f32 v182, v212, v213\n"
    "v_mfma_f32_16x16x32_bf16 v[208:211], a[124:127], a[92:95], v[208:211]\n"
    "v_exp_f32 v215, v215\n"
    "s_nop 0\n"
    "v_cvt_pk_bf16_f32 v183, v214, v215\n"
    "v_exp_f32 v216, v216\n"
    "v_mfma_f32_16x16x32_bf16 v[212:215], a[128:131], a[80:83], 0\n"
    "v_exp_f32 v217, v217\n"
    "v_mfma_f32_16x16x32_bf16 v[212:215], a[132:135], a[84:87], v[212:215]\n"
    "v_exp_f32 v218, v218\n"
    "v_mfma_f32_16x16x32_bf16 v[212:215], a[136:139], a[88:91], v[212:215]\n"
    "v_cvt_pk_bf16_f32 v188, v216, v217\n"
    "v_mfma_f32_16x16x32_bf16 v[212:215], a[140:143], a[92:95], v[212:215]\n"
    "v_exp_f32 v219, v219\n"
    "s_nop 0\n"
    "v_cvt_pk_bf16_f32 v189, v218, v219\n"
    "v_exp_f32 v220, v220\n"
    "v_mfma_f32_16x16x32_bf16 v[216:219], a[112:115], a[96:99], 0\n"
    "v_exp_f32 v221, v221\n"
    "v_mfma_f32_16x16x32_bf16 v[216:219], a[116:119], a[100:103], v[216:219]\n"
    "v_exp_f32 v222, v222\n"
    "v_mfma_f32_16x16x32_bf16 v[216:219], a[120:123], a[104:107], v[216:219]\n"
    "v_cvt_pk_bf16_f32 v190, v220, v221\n"
    "v_mfma_f32_16x16x32_bf16 v[216:219], a[124:127], a[108:111], v[216:219]\n"
    "v_exp_f32 v223, v223\n"
    "s_nop 0\n"
    "v_cvt_pk_bf16_f32 v191, v222, v223\n"
    "v_exp_f32 v208, v208\n"
    "v_mfma_f32_16x16x32_bf16 v[220:223], a[128:131], a[96:99], 0\n"
    "v_exp_f32 v209, v209\n"
    "v_mfma_f32_16x16x32_bf16 v[220:223], a[132:135], a[100:103], v[220:223]\n"
    "v_exp_f32 v210, v210\n"
    "v_mfma_f32_16x16x32_bf16 v[220:223], a[136:139], a[104:107], v[220:223]\n"
    "v_cvt_pk_bf16_f32 v196, v208, v209\n"
    "v_mfma_f32_16x16x32_bf16 v[220:223], a[140:143], a[108:111], v[220:223]\n"
    "s_waitcnt vmcnt(8)\n"
    "s_barrier\n"
    "v_exp_f32 v211, v211\n"
    "ds_read_b128 a[112:115], v232 offset:0\n"
    "v_cvt_pk_bf16_f32 v197, v210, v211\n"
    "v_exp_f32 v212, v212\n"
    "v_mfma_f32_16x16x32_bf16 v[208:211], a[144:147], a[32:35], 0\n"
    "s_add_u32 m0, s37, 114688\n"
    "v_exp_f32 v213, v213\n"
    "v_mfma_f32_16x16x32_bf16 v[208:211], a[148:151], a[36:39], v[208:211]\n"
    "v_exp_f32 v214, v214\n"
    "v_mfma_f32_16x16x32_bf16 v[208:211], a[152:155], a[40:43], v[208:211]\n"
    "ds_read_b128 a[116:119], v233 offset:0\n"
    "v_cvt_pk_bf16_f32 v198, v212, v213\n"
    "v_mfma_f32_16x16x32_bf16 v[208:211], a[156:159], a[44:47], v[208:211]\n"
    "v_exp_f32 v215, v215\n"
    "ds_read_b128 a[120:123], v232 offset:1024\n"
    "v_cvt_pk_bf16_f32 v199, v214, v215\n"
    "v_exp_f32 v216, v216\n"
    "v_mfma_f32_16x16x32_bf16 v[212:215], a[160:163], a[32:35], 0\n"
    "v_exp_f32 v217, v217\n"
    "v_mfma_f32_16x16x32_bf16 v[212:215], a[164:167], a[36:39], v[212:215]\n"
    "s_waitcnt lgkmcnt(1)\n"
    "v_exp_f32 v218, v218\n"
    "v_mfma_f32_16x16x32_bf16 v[212:215], a[168:171], a[40:43], v[212:215]\n"
    "ds_read_b128 a[124:127], v233 offset:1024\n"
    "v_cvt_pk_bf16_f32 v204, v216, v217\n"
    "v_mfma_f32_16x16x32_bf16 v[212:215], a[172:175], a[44:47], v[212:215]\n"
    "v_exp_f32 v219, v219\n"
    "buffer_load_dwordx4 v236, s[28:31], s43 offen sc1 lds\n"
    "ds_read_b128 a[128:131], v232 offset:4096\n"
    "v_cvt_pk_bf16_f32 v205, v218, v219\n"
    "v_mfma_f32_16x16x32_bf16 v[216:219], a[144:147], a[48:51], 0\n"
    "v_exp_f32 v220, v220\n"
    "v_mfma_f32_16x16x32_bf16 v[216:219], a[148:151], a[52:55], v[216:219]\n"
    "v_exp_f32 v221, v221\n"
    "v_mfma_f32_16x16x32_bf16 v[216:219], a[152:155], a[56:59], v[216:219]\n"
    "v_exp_f32 v222, v222\n"
    "v_mfma_f32_16x16x32_bf16 v[216:219], a[156:159], a[60:63], v[216:219]\n"
    "ds_read_b128 a[132:135], v233 offset:4096\n"
    "v_cvt_pk_bf16_f32 v206, v220, v221\n"
    "buffer_load_dwordx4 v236, s[28:31], s43 offen offset:2048 sc1 lds\n"
    "v_exp_f32 v223, v223\n"
    "s_add_u32 m0, s37, 115712\n"
    "ds_read_b128 a[136:139], v232 offset:5120\n"
    "v_cvt_pk_bf16_f32 v207, v222, v223\n"
    "v_exp_f32 v208, v208\n"
    "v_mfma_f32_16x16x32_bf16 v[220:223], a[160:163], a[48:51], 0\n"
    "v_exp_f32 v209, v209\n"
    "v_mfma_f32_16x16x32_bf16 v[220:223], a[164:167], a[52:55], v[220:223]\n"
    "v_exp_f32 v210, v210\n"
    "v_mfma_f32_16x16x32_bf16 v[220:223], a[168:171], a[56:59], v[220:223]\n"
    "ds_read_b128 a[140:143], v233 offset:5120\n"
    "v_cvt_pk_bf16_f32 v176, v208, v209\n"
    "v_mfma_f32_16x16x32_bf16 v[220:223], a[172:175], a[60:63], v[220:223]\n"
    "v_exp_f32 v211, v211\n"
    "buffer_load_dwordx4 v237, s[28:31], s43 offen sc1 lds\n"
    "ds_read_b64_tr_b16 a[176:177], v228 offset:16384\n"
    "v_cvt_pk_bf16_f32 v177, v210, v211\n"
    "v_mfma_f32_16x16x32_bf16 v[208:211], a[144:147], a[64:67], 0\n"
    "v_exp_f32 v212, v212\n"
    "v_mfma_f32_16x16x32_bf16 v[208:211], a[148:151], a[68:71], v[208:211]\n"
    "v_exp_f32 v213, v213\n"
    "v_mfma_f32_16x16x32_bf16 v[208:211], a[152:155], a[72:75], v[208:211]\n"
    "buffer_load_dwordx4 v237, s[28:31], s43 offen offset:2048 sc1 lds\n"
    "v_mfma_f32_16x16x32_bf16 v[208:211], a[156:159], a[76:79], v[208:211]\n"
    "s_add_u32 m0, s37, 0\n"
    "v_exp_f32 v214, v214\n"
    "ds_read_b64_tr_b16 a[178:179], v228 offset:20480\n"
    "v_cvt_pk_bf16_f32 v178, v212, v213\n"
    "v_exp_f32 v215, v215\n"
    "ds_read_b64_tr_b16 a[180:181], v230 offset:16384\n"
    "v_cvt_pk_bf16_f32 v179, v214, v215\n"
    "v_exp_f32 v216, v216\n"
    "v_mfma_f32_16x16x32_bf16 v[212:215], a[160:163], a[64:67], 0\n"
    "v_exp_f32 v217, v217\n"
    "v_mfma_f32_16x16x32_bf16 v[212:215], a[164:167], a[68:71], v[212:215]\n"
    "v_exp_f32 v218, v218\n"
    "v_mfma_f32_16x16x32_bf16 v[212:215], a[168:171], a[72:75], v[212:215]\n"
    "ds_read_b64_tr_b16 a[182:183], v230 offset:20480\n"
    "v_cvt_pk_bf16_f32 v184, v216, v217\n"
    "v_mfma_f32_16x16x32_bf16 v[212:215], a[172:175], a[76:79], v[212:215]\n"
    "buffer_load_dwordx4 v236, s[24:27], s44 offen sc1 lds\n"
    "buffer_load_dwordx4 v236, s[24:27], s44 offen offset:2048 sc1 lds\n"
    "v_exp_f32 v219, v219\n"
    "s_add_u32 m0, s37, 1024\n"
    "ds_read_b64_tr_b16 a[184:185], v228 offset:16392\n"
    "v_cvt_pk_bf16_f32 v185, v218, v219\n"
    "v_exp_f32 v220, v220\n"
    "v_mfma_f32_16x16x32_bf16 v[216:219], a[144:147], a[80:83], 0\n"
    "v_exp_f32 v221, v221\n"
    "v_mfma_f32_16x16x32_bf16 v[216:219], a[148:151], a[84:87], v[216:219]\n"
    "v_exp_f32 v222, v222\n"
    "v_mfma_f32_16x16x32_bf16 v[216:219], a[152:155], a[88:91], v[216:219]\n"
    "ds_read_b64_tr_b16 a[186:187], v228 offset:20488\n"
    "v_cvt_pk_bf16_f32 v186, v220, v221\n"
    "v_mfma_f32_16x16x32_bf16 v[216:219], a[156:159], a[92:95], v[216:219]\n"
    "v_exp_f32 v223, v223\n"
    "ds_read_b64_tr_b16 a[188:189], v230 offset:16392\n"
    "v_cvt_pk_bf16_f32 v187, v222, v223\n"
    "v_exp_f32 v208, v208\n"
    "v_mfma_f32_16x16x32_bf16 v[220:223], a[160:163], a[80:83], 0\n"
    "v_exp_f32 v209, v209\n"
    "v_mfma_f32_16x16x32_bf16 v[220:223], a[164:167], a[84:87], v[220:223]\n"
    "v_exp_f32 v210, v210\n"
    "v_mfma_f32_16x16x32_bf16 v[220:223], a[168:171], a[88:91], v[220:223]\n"
    "ds_read_b64_tr_b16 a[190:191], v230 offset:20488\n"
    "v_cvt_pk_bf16_f32 v192, v208, v209\n"
    "v_mfma_f32_16x16x32_bf16 v[220:223], a[172:175], a[92:95], v[220:223]\n"
    "buffer_load_dwordx4 v237, s[24:27], s44 offen sc1 lds\n"
    "buffer_load_dwordx4 v237, s[24:27], s44 offen offset:2048 sc1 lds\n"
    "v_exp_f32 v211, v211\n"
    "ds_read_b64_tr_b16 a[192:193], v229 offset:16384\n"
    "v_cvt_pk_bf16_f32 v193, v210, v211\n"
    "v_exp_f32 v212, v212\n"
    "ds_read_b128 a[144:147], v234 offset:0\n"
    "ds_read_b128 a[148:151], v235 offset:0\n"
    "v_exp_f32 v213, v213\n"
    "ds_read_b128 a[152:155], v234 offset:1024\n"
    "v_exp_f32 v214, v214\n"
    "v_cvt_pk_bf16_f32 v194, v212, v213\n"
    "ds_read_b128 a[156:159], v235 offset:1024\n"
    "ds_read_b128 a[160:163], v234 offset:4096\n"
    "v_exp_f32 v215, v215\n"
    "ds_read_b128 a[164:167], v235 offset:4096\n"
    "v_cvt_pk_bf16_f32 v195, v214, v215\n"
    "v_exp_f32 v216, v216\n"
    "ds_read_b128 a[168:171], v234 offset:5120\n"
    "ds_read_b128 a[172:175], v235 offset:5120\n"
    "v_exp_f32 v217, v217\n"
    "v_exp_f32 v218, v218\n"
    "v_cvt_pk_bf16_f32 v200, v216, v217\n"
    "v_exp_f32 v219, v219\n"
    "s_nop 0\n"
    "v_cvt_pk_bf16_f32 v201, v218, v219\n"
    "v_exp_f32 v220, v220\n"
    "v_exp_f32 v221, v221\n"
    "v_exp_f32 v222, v222\n"
    "v_cvt_pk_bf16_f32 v202, v220, v221\n"
    "1:\n"
    "v_mfma_f32_16x16x32_bf16 v[208:211], a[112:115], a[32:35], 0\n"
    "v_exp_f32 v223, v223\n"
    "v_mfma_f32_16x16x32_bf16 v[208:211], a[116:119], a[36:39], v[208:211]\n"
    "s_waitcnt lgkmcnt(0)\n"
    "ds_read_b64_tr_b16 a[194:195], v229 offset:20480\n"
    "v_cvt_pk_bf16_f32 v203, v222, v223\n"
    "v_mfma_f32_16x16x32_bf16 v[208:211], a[120:123], a[40:43], v[208:211]\n"
    "ds_read_b64_tr_b16 a[196:197], v231 offset:16384\n"
    "ds_read_b64_tr_b16 a[198:199], v231 offset:20480\n"
    "v_mfma_f32_16x16x32_bf16 v[208:211], a[124:127], a[44:47], v[208:211]\n"
    "ds_read_b64_tr_b16 a[200:201], v229 offset:16392\n"
    "ds_read_b64_tr_b16 a[202:203], v229 offset:20488\n"
    "v_mfma_f32_16x16x32_bf16 v[212:215], a[128:131], a[32:35], 0\n"
    "ds_read_b64_tr_b16 a[204:205], v231 offset:16392\n"
    "ds_read_b64_tr_b16 a[206:207], v231 offset:20488\n"
    "v_mfma_f32_16x16x32_bf16 v[212:215], a[132:135], a[36:39], v[212:215]\n"
    "v_mfma_f32_16x16x32_bf16 v[212:215], a[136:139], a[40:43], v[212:215]\n"
    "v_mfma_f32_16x16x32_bf16 v[212:215], a[140:143], a[44:47], v[212:215]\n"
    "v_exp_f32 v208, v208\n"
    "v_mfma_f32_16x16x32_bf16 v[8:11], a[176:179], v[172:175], v[8:11]\n"
    "v_exp_f32 v209, v209\n"
    "v_mfma_f32_16x16x32_bf16 v[8:11], a[180:183], v[176:179], v[8:11]\n"
    "v_exp_f32 v210, v210\n"
    "v_mfma_f32_16x16x32_bf16 v[12:15], a[184:187], v[172:175], v[12:15]\n"
    "v_cvt_pk_bf16_f32 v136, v208, v209\n"
    "v_mfma_f32_16x16x32_bf16 v[12:15], a[188:191], v[176:179], v[12:15]\n"
    "v_exp_f32 v211, v211\n"
    "v_mfma_f32_16x16x32_bf16 a[208:211], a[228:231], v[172:175], a[208:211]\n"
    "v_cvt_pk_bf16_f32 v137, v210, v211\n"
    "v_mfma_f32_16x16x32_bf16 a[208:211], a[228:231], v[176:179], a[208:211]\n"
    "v_exp_f32 v212, v212\n"
    "v_mfma_f32_16x16x32_bf16 a[0:3], a[176:179], v[204:207], a[0:3]\n"
    "v_exp_f32 v213, v213\n"
    "v_mfma_f32_16x16x32_bf16 v[40:43], a[176:179], v[180:183], v[40:43]\n"
    "v_exp_f32 v214, v214\n"
    "v_mfma_f32_16x16x32_bf16 v[40:43], a[180:183], v[184:187], v[40:43]\n"
    "v_cvt_pk_bf16_f32 v138, v212, v213\n"
    "v_mfma_f32_16x16x32_bf16 v[216:219], a[112:115], a[48:51], 0\n"
    "v_exp_f32 v215, v215\n"
    "v_mfma_f32_16x16x32_bf16 v[216:219], a[116:119], a[52:55], v[216:219]\n"
    "v_cvt_pk_bf16_f32 v139, v214, v215\n"
    "v_mfma_f32_16x16x32_bf16 v[216:219], a[120:123], a[56:59], v[216:219]\n"
    "v_mfma_f32_16x16x32_bf16 v[216:219], a[124:127], a[60:63], v[216:219]\n"
    "v_mfma_f32_16x16x32_bf16 v[220:223], a[128:131], a[48:51], 0\n"
    "v_mfma_f32_16x16x32_bf16 v[220:223], a[132:135], a[52:55], v[220:223]\n"
    "v_mfma_f32_16x16x32_bf16 v[220:223], a[136:139], a[56:59], v[220:223]\n"
    "v_mfma_f32_16x16x32_bf16 v[220:223], a[140:143], a[60:63], v[220:223]\n"
    "v_exp_f32 v216, v216\n"
    "v_mfma_f32_16x16x32_bf16 v[44:47], a[184:187], v[180:183], v[44:47]\n"
    "v_exp_f32 v217, v217\n"
    "v_mfma_f32_16x16x32_bf16 v[44:47], a[188:191], v[184:187], v[44:47]\n"
    "v_exp_f32 v218, v218\n"
    "v_mfma_f32_16x16x32_bf16 a[212:215], a[228:231], v[180:183], a[212:215]\n"
    "v_cvt_pk_bf16_f32 v144, v216, v217\n"
    "v_mfma_f32_16x16x32_bf16 a[212:215], a[228:231], v[184:187], a[212:215]\n"
    "v_exp_f32 v219, v219\n"
    "v_mfma_f32_16x16x32_bf16 v[72:75], a[176:179], v[188:191], v[72:75]\n"
    "v_cvt_pk_bf16_f32 v145, v218, v219\n"
    "v_mfma_f32_16x16x32_bf16 v[72:75], a[180:183], v[192:195], v[72:75]\n"
    "v_exp_f32 v220, v220\n"
    "v_mfma_f32_16x16x32_bf16 a[4:7], a[184:187], v[204:207], a[4:7]\n"
    "v_exp_f32 v221, v221\n"
    "v_mfma_f32_16x16x32_bf16 v[76:79], a[184:187], v[188:191], v[76:79]\n"
    "v_exp_f32 v222, v222\n"
    "v_mfma_f32_16x16x32_bf16 v[76:79], a[188:191], v[192:195], v[76:79]\n"
    "v_cvt_pk_bf16_f32 v146, v220, v221\n"
    "v_mfma_f32_16x16x32_bf16 v[208:211], a[112:115], a[64:67], 0\n"
    "v_exp_f32 v223, v223\n"
    "v_mfma_f32_16x16x32_bf16 v[208:211], a[116:119], a[68:71], v[208:211]\n"
    "v_cvt_pk_bf16_f32 v147, v222, v223\n"
    "v_mfma_f32_16x16x32_bf16 v[208:211], a[120:123], a[72:75], v[208:211]\n"
    "v_mfma_f32_16x16x32_bf16 v[208:211], a[124:127], a[76:79], v[208:211]\n"
    "v_mfma_f32_16x16x32_bf16 v[212:215], a[128:131], a[64:67], 0\n"
    "v_mfma_f32_16x16x32_bf16 v[212:215], a[132:135], a[68:71], v[212:215]\n"
    "v_mfma_f32_16x16x32_bf16 v[212:215], a[136:139], a[72:75], v[212:215]\n"
    "v_mfma_f32_16x16x32_bf16 v[212:215], a[140:143], a[76:79], v[212:215]\n"
    "v_exp_f32 v208, v208\n"
    "v_mfma_f32_16x16x32_bf16 a[216:219], a[228:231], v[188:191], a[216:219]\n"
    "v_exp_f32 v209, v209\n"
    "v_mfma_f32_16x16x32_bf16 a[216:219], a[228:231], v[192:195], a[216:219]\n"
    "v_exp_f32 v210, v210\n"
    "v_mfma_f32_16x16x32_bf16 v[104:107], a[176:179], v[196:199], v[104:107]\n"
    "v_cvt_pk_bf16_f32 v152, v208, v209\n"
    "v_mfma_f32_16x16x32_bf16 v[104:107], a[180:183], v[200:203], v[104:107]\n"
    "v_exp_f32 v211, v211\n"
    "v_mfma_f32_16x16x32_bf16 v[108:111], a[184:187], v[196:199], v[108:111]\n"
    "ds_read_b64_tr_b16 a[176:177], v228 offset:17408\n"
    "v_cvt_pk_bf16_f32 v153, v210, v211\n"
    "v_mfma_f32_16x16x32_bf16 v[108:111], a[188:191], v[200:203], v[108:111]\n"
    "v_exp_f32 v212, v212\n"
    "v_mfma_f32_16x16x32_bf16 a[224:227], a[228:231], v[204:207], a[224:227]\n"
    "v_exp_f32 v213, v213\n"
    "v_mfma_f32_16x16x32_bf16 a[220:223], a[228:231], v[196:199], a[220:223]\n"
    "v_exp_f32 v214, v214\n"
    "v_mfma_f32_16x16x32_bf16 a[220:223], a[228:231], v[200:203], a[220:223]\n"
    "ds_read_b64_tr_b16 a[178:179], v228 offset:21504\n"
    "v_cvt_pk_bf16_f32 v154, v212, v213\n"
    "v_mfma_f32_16x16x32_bf16 v[216:219], a[112:115], a[80:83], 0\n"
    "v_exp_f32 v215, v215\n"
    "v_mfma_f32_16x16x32_bf16 v[216:219], a[116:119], a[84:87], v[216:219]\n"
    "ds_read_b64_tr_b16 a[180:181], v230 offset:17408\n"
    "v_cvt_pk_bf16_f32 v155, v214, v215\n"
    "v_mfma_f32_16x16x32_bf16 v[216:219], a[120:123], a[88:91], v[216:219]\n"
    "ds_read_b64_tr_b16 a[182:183], v230 offset:21504\n"
    "ds_read_b64_tr_b16 a[184:185], v228 offset:17416\n"
    "v_mfma_f32_16x16x32_bf16 v[216:219], a[124:127], a[92:95], v[216:219]\n"
    "ds_read_b64_tr_b16 a[186:187], v228 offset:21512\n"
    "ds_read_b64_tr_b16 a[188:189], v230 offset:17416\n"
    "v_mfma_f32_16x16x32_bf16 v[220:223], a[128:131], a[80:83], 0\n"
    "ds_read_b64_tr_b16 a[190:191], v230 offset:21512\n"
    "v_mfma_f32_16x16x32_bf16 v[220:223], a[132:135], a[84:87], v[220:223]\n"
    "v_mfma_f32_16x16x32_bf16 v[220:223], a[136:139], a[88:91], v[220:223]\n"
    "v_mfma_f32_16x16x32_bf16 v[220:223], a[140:143], a[92:95], v[220:223]\n"
    "s_waitcnt lgkmcnt(1)\n"
    "v_exp_f32 v216, v216\n"
    "v_mfma_f32_16x16x32_bf16 v[16:19], a[192:195], v[172:175], v[16:19]\n"
    "v_exp_f32 v217, v217\n"
    "v_mfma_f32_16x16x32_bf16 v[16:19], a[196:199], v[176:179], v[16:19]\n"
    "v_exp_f32 v218, v218\n"
    "v_mfma_f32_16x16x32_bf16 v[20:23], a[200:203], v[172:175], v[20:23]\n"
    "v_cvt_pk_bf16_f32 v160, v216, v217\n"
    "v_mfma_f32_16x16x32_bf16 v[20:23], a[204:207], v[176:179], v[20:23]\n"
    "v_exp_f32 v219, v219\n"
    "v_mfma_f32_16x16x32_bf16 v[48:51], a[192:195], v[180:183], v[48:51]\n"
    "v_cvt_pk_bf16_f32 v161, v218, v219\n"
    "v_mfma_f32_16x16x32_bf16 v[48:51], a[196:199], v[184:187], v[48:51]\n"
    "v_exp_f32 v220, v220\n"
    "v_mfma_f32_16x16x32_bf16 a[8:11], a[192:195], v[204:207], a[8:11]\n"
    "v_exp_f32 v221, v221\n"
    "v_mfma_f32_16x16x32_bf16 v[52:55], a[200:203], v[180:183], v[52:55]\n"
    "v_exp_f32 v222, v222\n"
    "v_mfma_f32_16x16x32_bf16 v[52:55], a[204:207], v[184:187], v[52:55]\n"
    "v_cvt_pk_bf16_f32 v162, v220, v221\n"
    "v_mfma_f32_16x16x32_bf16 v[208:211], a[112:115], a[96:99], 0\n"
    "v_exp_f32 v223, v223\n"
    "v_mfma_f32_16x16x32_bf16 v[208:211], a[116:119], a[100:103], v[208:211]\n"
    "v_cvt_pk_bf16_f32 v163, v222, v223\n"
    "v_mfma_f32_16x16x32_bf16 v[208:211], a[120:123], a[104:107], v[208:211]\n"
    "v_mfma_f32_16x16x32_bf16 v[208:211], a[124:127], a[108:111], v[208:211]\n"
    "v_mfma_f32_16x16x32_bf16 v[212:215], a[128:131], a[96:99], 0\n"
    "v_mfma_f32_16x16x32_bf16 v[212:215], a[132:135], a[100:103], v[212:215]\n"
    "v_mfma_f32_16x16x32_bf16 v[212:215], a[136:139], a[104:107], v[212:215]\n"
    "v_mfma_f32_16x16x32_bf16 v[212:215], a[140:143], a[108:111], v[212:215]\n"
    "v_exp_f32 v208, v208\n"
    "v_mfma_f32_16x16x32_bf16 v[80:83], a[192:195], v[188:191], v[80:83]\n"
    "v_exp_f32 v209, v209\n"
    "v_mfma_f32_16x16x32_bf16 v[80:83], a[196:199], v[192:195], v[80:83]\n"
    "v_exp_f32 v210, v210\n"
    "v_mfma_f32_16x16x32_bf16 v[84:87], a[200:203], v[188:191], v[84:87]\n"
    "v_cvt_pk_bf16_f32 v168, v208, v209\n"
    "v_mfma_f32_16x16x32_bf16 v[84:87], a[204:207], v[192:195], v[84:87]\n"
    "v_exp_f32 v211, v211\n"
    "v_mfma_f32_16x16x32_bf16 v[112:115], a[192:195], v[196:199], v[112:115]\n"
    "v_cvt_pk_bf16_f32 v169, v210, v211\n"
    "v_mfma_f32_16x16x32_bf16 v[112:115], a[196:199], v[200:203], v[112:115]\n"
    "v_exp_f32 v212, v212\n"
    "v_mfma_f32_16x16x32_bf16 a[12:15], a[200:203], v[204:207], a[12:15]\n"
    "v_exp_f32 v213, v213\n"
    "v_mfma_f32_16x16x32_bf16 v[116:119], a[200:203], v[196:199], v[116:119]\n"
    "v_exp_f32 v214, v214\n"
    "v_mfma_f32_16x16x32_bf16 v[116:119], a[204:207], v[200:203], v[116:119]\n"
    "s_waitcnt vmcnt(8)\n"
    "s_barrier\n"
    "ds_read_b64_tr_b16 a[192:193], v229 offset:17408\n"
    "v_cvt_pk_bf16_f32 v170, v212, v213\n"
    "v_mfma_f32_16x16x32_bf16 v[216:219], a[144:147], a[32:35], 0\n"
    "s_add_u32 m0, s68, 0\n"
    "v_exp_f32 v215, v215\n"
    "v_mfma_f32_16x16x32_bf16 v[216:219], a[148:151], a[36:39], v[216:219]\n"
    "ds_read_b64_tr_b16 a[194:195], v229 offset:21504\n"
    "v_cvt_pk_bf16_f32 v171, v214, v215\n"
    "v_mfma_f32_16x16x32_bf16 v[216:219], a[152:155], a[40:43], v[216:219]\n"
    "ds_read_b64_tr_b16 a[196:197], v231 offset:17408\n"
    "ds_read_b64_tr_b16 a[198:199], v231 offset:21504\n"
    "v_mfma_f32_16x16x32_bf16 v[216:219], a[156:159], a[44:47], v[216:219]\n"
    "ds_read_b64_tr_b16 a[200:201], v229 offset:17416\n"
    "ds_read_b64_tr_b16 a[202:203], v229 offset:21512\n"
    "v_mfma_f32_16x16x32_bf16 v[220:223], a[160:163], a[32:35], 0\n"
    "buffer_load_dwordx4 v236, s[28:31], s72 offen sc1 lds\n"
    "v_mfma_f32_16x16x32_bf16 v[220:223], a[164:167], a[36:39], v[220:223]\n"
    "ds_read_b64_tr_b16 a[204:205], v231 offset:17416\n"
    "ds_read_b64_tr_b16 a[206:207], v231 offset:21512\n"
    "v_mfma_f32_16x16x32_bf16 v[220:223], a[168:171], a[40:43], v[220:223]\n"
    "ds_read_b128 a[112:115], v232 offset:16384\n"
    "ds_read_b128 a[116:119], v233 offset:16384\n"
    "v_mfma_f32_16x16x32_bf16 v[220:223], a[172:175], a[44:47], v[220:223]\n"
    "v_exp_f32 v216, v216\n"
    "v_mfma_f32_16x16x32_bf16 v[24:27], a[176:179], v[172:175], v[24:27]\n"
    "v_exp_f32 v217, v217\n"
    "v_mfma_f32_16x16x32_bf16 v[24:27], a[180:183], v[176:179], v[24:27]\n"
    "buffer_load_dwordx4 v236, s[28:31], s72 offen offset:2048 sc1 lds\n"
    "v_mfma_f32_16x16x32_bf16 v[28:31], a[184:187], v[172:175], v[28:31]\n"
    "s_waitcnt lgkmcnt(0)\n"
    "v_exp_f32 v218, v218\n"
    "v_mfma_f32_16x16x32_bf16 v[28:31], a[188:191], v[176:179], v[28:31]\n"
    "ds_read_b128 a[120:123], v232 offset:17408\n"
    "v_cvt_pk_bf16_f32 v140, v216, v217\n"
    "v_mfma_f32_16x16x32_bf16 v[56:59], a[176:179], v[180:183], v[56:59]\n"
    "v_exp_f32 v219, v219\n"
    "v_mfma_f32_16x16x32_bf16 v[56:59], a[180:183], v[184:187], v[56:59]\n"
    "ds_read_b128 a[124:127], v233 offset:17408\n"
    "v_cvt_pk_bf16_f32 v141, v218, v219\n"
    "v_mfma_f32_16x16x32_bf16 a[16:19], a[176:179], v[204:207], a[16:19]\n"
    "s_add_u32 m0, s68, 1024\n"
    "v_exp_f32 v220, v220\n"
    "v_mfma_f32_16x16x32_bf16 v[60:63], a[184:187], v[180:183], v[60:63]\n"
    "v_exp_f32 v221, v221\n"
    "v_mfma_f32_16x16x32_bf16 v[60:63], a[188:191], v[184:187], v[60:63]\n"
    "v_exp_f32 v222, v222\n"
    "v_mfma_f32_16x16x32_bf16 v[208:211], a[144:147], a[48:51], 0\n"
    "ds_read_b128 a[128:131], v232 offset:20480\n"
    "v_cvt_pk_bf16_f32 v142, v220, v221\n"
    "v_mfma_f32_16x16x32_bf16 v[208:211], a[148:151], a[52:55], v[208:211]\n"
    "v_exp_f32 v223, v223\n"
    "v_mfma_f32_16x16x32_bf16 v[208:211], a[152:155], a[56:59], v[208:211]\n"
    "buffer_load_dwordx4 v237, s[28:31], s72 offen sc1 lds\n"
    "v_mfma_f32_16x16x32_bf16 v[208:211], a[156:159], a[60:63], v[208:211]\n"
    "ds_read_b128 a[132:135], v233 offset:20480\n"
    "v_cvt_pk_bf16_f32 v143, v222, v223\n"
    "v_mfma_f32_16x16x32_bf16 v[212:215], a[160:163], a[48:51], 0\n"
    "ds_read_b128 a[136:139], v232 offset:21504\n"
    "ds_read_b128 a[140:143], v233 offset:21504\n"
    "v_mfma_f32_16x16x32_bf16 v[212:215], a[164:167], a[52:55], v[212:215]\n"
    "v_mfma_f32_16x16x32_bf16 v[212:215], a[168:171], a[56:59], v[212:215]\n"
    "v_mfma_f32_16x16x32_bf16 v[212:215], a[172:175], a[60:63], v[212:215]\n"
    "buffer_load_dwordx4 v237, s[28:31], s72 offen offset:2048 sc1 lds\n"
    "v_mfma_f32_16x16x32_bf16 v[88:91], a[176:179], v[188:191], v[88:91]\n"
    "v_exp_f32 v208, v208\n"
    "v_mfma_f32_16x16x32_bf16 v[88:91], a[180:183], v[192:195], v[88:91]\n"
    "v_exp_f32 v209, v209\n"
    "v_mfma_f32_16x16x32_bf16 v[92:95], a[184:187], v[188:191], v[92:95]\n"
    "v_exp_f32 v210, v210\n"
    "v_mfma_f32_16x16x32_bf16 v[92:95], a[188:191], v[192:195], v[92:95]\n"
    "v_cvt_pk_bf16_f32 v148, v208, v209\n"
    "v_mfma_f32_16x16x32_bf16 v[120:123], a[176:179], v[196:199], v[120:123]\n"
    "s_add_u32 m0, s69, 0\n"
    "v_exp_f32 v211, v211\n"
    "v_mfma_f32_16x16x32_bf16 v[120:123], a[180:183], v[200:203], v[120:123]\n"
    "ds_read_b64_tr_b16 a[176:177], v4 offset:0\n"
    "v_cvt_pk_bf16_f32 v149, v210, v211\n"
    "v_mfma_f32_16x16x32_bf16 a[20:23], a[184:187], v[204:207], a[20:23]\n"
    "v_exp_f32 v212, v212\n"
    "v_mfma_f32_16x16x32_bf16 v[124:127], a[184:187], v[196:199], v[124:127]\n"
    "v_exp_f32 v213, v213\n"
    "v_mfma_f32_16x16x32_bf16 v[124:127], a[188:191], v[200:203], v[124:127]\n"
    "buffer_load_dwordx4 v236, s[24:27], s73 offen sc1 lds\n"
    "v_mfma_f32_16x16x32_bf16 v[216:219], a[144:147], a[64:67], 0\n"
    "v_exp_f32 v214, v214\n"
    "v_mfma_f32_16x16x32_bf16 v[216:219], a[148:151], a[68:71], v[216:219]\n"
    "ds_read_b64_tr_b16 a[178:179], v4 offset:4096\n"
    "v_cvt_pk_bf16_f32 v150, v212, v213\n"
    "v_mfma_f32_16x16x32_bf16 v[216:219], a[152:155], a[72:75], v[216:219]\n"
    "v_exp_f32 v215, v215\n"
    "v_mfma_f32_16x16x32_bf16 v[216:219], a[156:159], a[76:79], v[216:219]\n"
    "ds_read_b64_tr_b16 a[180:181], v6 offset:0\n"
    "v_cvt_pk_bf16_f32 v151, v214, v215\n"
    "v_mfma_f32_16x16x32_bf16 v[220:223], a[160:163], a[64:67], 0\n"
    "buffer_load_dwordx4 v236, s[24:27], s73 offen offset:2048 sc1 lds\n"
    "v_mfma_f32_16x16x32_bf16 v[220:223], a[164:167], a[68:71], v[220:223]\n"
    "ds_read_b64_tr_b16 a[182:183], v6 offset:4096\n"
    "ds_read_b64_tr_b16 a[184:185], v4 offset:8\n"
    "v_mfma_f32_16x16x32_bf16 v[220:223], a[168:171], a[72:75], v[220:223]\n"
    "ds_read_b64_tr_b16 a[186:187], v4 offset:4104\n"
    "ds_read_b64_tr_b16 a[188:189], v6 offset:8\n"
    "v_mfma_f32_16x16x32_bf16 v[220:223], a[172:175], a[76:79], v[220:223]\n"
    "v_exp_f32 v216, v216\n"
    "v_mfma_f32_16x16x32_bf16 v[32:35], a[192:195], v[172:175], v[32:35]\n"
    "v_exp_f32 v217, v217\n"
    "v_mfma_f32_16x16x32_bf16 v[32:35], a[196:199], v[176:179], v[32:35]\n"
    "s_add_u32 m0, s69, 1024\n"
    "v_exp_f32 v218, v218\n"
    "v_mfma_f32_16x16x32_bf16 v[36:39], a[200:203], v[172:175], v[36:39]\n"
    "ds_read_b64_tr_b16 a[190:191], v6 offset:4104\n"
    "v_cvt_pk_bf16_f32 v156, v216, v217\n"
    "v_mfma_f32_16x16x32_bf16 v[36:39], a[204:207], v[176:179], v[36:39]\n"
    "v_exp_f32 v219, v219\n"
    "v_mfma_f32_16x16x32_bf16 v[64:67], a[192:195], v[180:183], v[64:67]\n"
    "v_cvt_pk_bf16_f32 v157, v218, v219\n"
    "v_mfma_f32_16x16x32_bf16 v[64:67], a[196:199], v[184:187], v[64:67]\n"
    "v_exp_f32 v220, v220\n"
    "v_mfma_f32_16x16x32_bf16 a[24:27], a[192:195], v[204:207], a[24:27]\n"
    "buffer_load_dwordx4 v237, s[24:27], s73 offen sc1 lds\n"
    "v_mfma_f32_16x16x32_bf16 v[68:71], a[200:203], v[180:183], v[68:71]\n"
    "v_exp_f32 v221, v221\n"
    "v_mfma_f32_16x16x32_bf16 v[68:71], a[204:207], v[184:187], v[68:71]\n"
    "v_exp_f32 v222, v222\n"
    "v_mfma_f32_16x16x32_bf16 v[208:211], a[144:147], a[80:83], 0\n"
    "v_cvt_pk_bf16_f32 v158, v220, v221\n"
    "v_mfma_f32_16x16x32_bf16 v[208:211], a[148:151], a[84:87], v[208:211]\n"
    "v_exp_f32 v223, v223\n"
    "v_mfma_f32_16x16x32_bf16 v[208:211], a[152:155], a[88:91], v[208:211]\n"
    "buffer_load_dwordx4 v237, s[24:27], s73 offen offset:2048 sc1 lds\n"
    "v_mfma_f32_16x16x32_bf16 v[208:211], a[156:159], a[92:95], v[208:211]\n"
    "ds_read_b128 a[144:147], v234 offset:16384\n"
    "v_cvt_pk_bf16_f32 v159, v222, v223\n"
    "v_mfma_f32_16x16x32_bf16 v[212:215], a[160:163], a[80:83], 0\n"
    "ds_read_b128 a[148:151], v235 offset:16384\n"
    "ds_read_b128 a[152:155], v234 offset:17408\n"
    "v_mfma_f32_16x16x32_bf16 v[212:215], a[164:167], a[84:87], v[212:215]\n"
    "ds_read_b128 a[156:159], v235 offset:17408\n"
    "ds_read_b128 a[160:163], v234 offset:20480\n"
    "v_mfma_f32_16x16x32_bf16 v[212:215], a[168:171], a[88:91], v[212:215]\n"
    "ds_read_b128 a[164:167], v235 offset:20480\n"
    "v_mfma_f32_16x16x32_bf16 v[212:215], a[172:175], a[92:95], v[212:215]\n"
    "v_exp_f32 v208, v208\n"
    "v_mfma_f32_16x16x32_bf16 v[96:99], a[192:195], v[188:191], v[96:99]\n"
    "v_exp_f32 v209, v209\n"
    "v_mfma_f32_16x16x32_bf16 v[96:99], a[196:199], v[192:195], v[96:99]\n"
    "v_exp_f32 v210, v210\n"
    "v_mfma_f32_16x16x32_bf16 v[100:103], a[200:203], v[188:191], v[100:103]\n"
    "ds_read_b128 a[168:171], v234 offset:21504\n"
    "v_cvt_pk_bf16_f32 v164, v208, v209\n"
    "v_mfma_f32_16x16x32_bf16 v[100:103], a[204:207], v[192:195], v[100:103]\n"
    "v_exp_f32 v211, v211\n"
    "v_mfma_f32_16x16x32_bf16 v[128:131], a[192:195], v[196:199], v[128:131]\n"
    "ds_read_b128 a[172:175], v235 offset:21504\n"
    "v_cvt_pk_bf16_f32 v165, v210, v211\n"
    "v_mfma_f32_16x16x32_bf16 v[128:131], a[196:199], v[200:203], v[128:131]\n"
    "v_exp_f32 v212, v212\n"
    "v_mfma_f32_16x16x32_bf16 a[28:31], a[200:203], v[204:207], a[28:31]\n"
    "v_exp_f32 v213, v213\n"
    "v_mfma_f32_16x16x32_bf16 v[132:135], a[200:203], v[196:199], v[132:135]\n"
    "v_exp_f32 v214, v214\n"
    "v_mfma_f32_16x16x32_bf16 v[132:135], a[204:207], v[200:203], v[132:135]\n"
    "ds_read_b64_tr_b16 a[192:193], v5 offset:0\n"
    "v_cvt_pk_bf16_f32 v166, v212, v213\n"
    "s_cmp_eq_u32 s75, 1\n"
    "s_cbranch_scc1 4f\n"
    "v_mfma_f32_16x16x32_bf16 v[216:219], a[112:115], a[32:35], 0\n"
    "v_exp_f32 v215, v215\n"
    "v_mfma_f32_16x16x32_bf16 v[216:219], a[116:119], a[36:39], v[216:219]\n"
    "s_waitcnt lgkmcnt(1)\n"
    "ds_read_b64_tr_b16 a[194:195], v5 offset:4096\n"
    "v_cvt_pk_bf16_f32 v167, v214, v215\n"
    "v_mfma_f32_16x16x32_bf16 v[216:219], a[120:123], a[40:43], v[216:219]\n"
    "ds_read_b64_tr_b16 a[196:197], v7 offset:0\n"
    "ds_read_b64_tr_b16 a[198:199], v7 offset:4096\n"
    "v_mfma_f32_16x16x32_bf16 v[216:219], a[124:127], a[44:47], v[216:219]\n"
    "ds_read_b64_tr_b16 a[200:201], v5 offset:8\n"
    "ds_read_b64_tr_b16 a[202:203], v5 offset:4104\n"
    "v_mfma_f32_16x16x32_bf16 v[220:223], a[128:131], a[32:35], 0\n"
    "ds_read_b64_tr_b16 a[204:205], v7 offset:8\n"
    "ds_read_b64_tr_b16 a[206:207], v7 offset:4104\n"
    "v_mfma_f32_16x16x32_bf16 v[220:223], a[132:135], a[36:39], v[220:223]\n"
    "v_mfma_f32_16x16x32_bf16 v[220:223], a[136:139], a[40:43], v[220:223]\n"
    "v_mfma_f32_16x16x32_bf16 v[220:223], a[140:143], a[44:47], v[220:223]\n"
    "v_exp_f32 v216, v216\n"
    "v_mfma_f32_16x16x32_bf16 v[8:11], a[176:179], v[136:139], v[8:11]\n"
    "v_exp_f32 v217, v217\n"
    "v_mfma_f32_16x16x32_bf16 v[8:11], a[180:183], v[140:143], v[8:11]\n"
    "v_exp_f32 v218, v218\n"
    "v_mfma_f32_16x16x32_bf16 v[12:15], a[184:187], v[136:139], v[12:15]\n"
    "v_cvt_pk_bf16_f32 v172, v216, v217\n"
    "v_mfma_f32_16x16x32_bf16 v[12:15], a[188:191], v[140:143], v[12:15]\n"
    "v_exp_f32 v219, v219\n"
    "v_mfma_f32_16x16x32_bf16 a[208:211], a[228:231], v[136:139], a[208:211]\n"
    "v_cvt_pk_bf16_f32 v173, v218, v219\n"
    "v_mfma_f32_16x16x32_bf16 a[208:211], a[228:231], v[140:143], a[208:211]\n"
    "v_exp_f32 v220, v220\n"
    "v_mfma_f32_16x16x32_bf16 a[0:3], a[176:179], v[168:171], a[0:3]\n"
    "v_exp_f32 v221, v221\n"
    "v_mfma_f32_16x16x32_bf16 v[40:43], a[176:179], v[144:147], v[40:43]\n"
    "v_exp_f32 v222, v222\n"
    "v_mfma_f32_16x16x32_bf16 v[40:43], a[180:183], v[148:151], v[40:43]\n"
    "v_cvt_pk_bf16_f32 v174, v220, v221\n"
    "v_mfma_f32_16x16x32_bf16 v[208:211], a[112:115], a[48:51], 0\n"
    "v_exp_f32 v223, v223\n"
    "v_mfma_f32_16x16x32_bf16 v[208:211], a[116:119], a[52:55], v[208:211]\n"
    "v_cvt_pk_bf16_f32 v175, v222, v223\n"
    "v_mfma_f32_16x16x32_bf16 v[208:211], a[120:123], a[56:59], v[208:211]\n"
    "v_mfma_f32_16x16x32_bf16 v[208:211], a[124:127], a[60:63], v[208:211]\n"
    "v_mfma_f32_16x16x32_bf16 v[212:215], a[128:131], a[48:51], 0\n"
    "v_mfma_f32_16x16x32_bf16 v[212:215], a[132:135], a[52:55], v[212:215]\n"
    "v_mfma_f32_16x16x32_bf16 v[212:215], a[136:139], a[56:59], v[212:215]\n"
    "v_mfma_f32_16x16x32_bf16 v[212:215], a[140:143], a[60:63], v[212:215]\n"
    "v_exp_f32 v208, v208\n"
    "v_mfma_f32_16x16x32_bf16 v[44:47], a[184:187], v[144:147], v[44:47]\n"
    "v_exp_f32 v209, v209\n"
    "v_mfma_f32_16x16x32_bf16 v[44:47], a[188:191], v[148:151], v[44:47]\n"
    "v_exp_f32 v210, v210\n"
    "v_mfma_f32_16x16x32_bf16 a[212:215], a[228:231], v[144:147], a[212:215]\n"
    "v_cvt_pk_bf16_f32 v180, v208, v209\n"
    "v_mfma_f32_16x16x32_bf16 a[212:215], a[228:231], v[148:151], a[212:215]\n"
    "v_exp_f32 v211, v211\n"
    "v_mfma_f32_16x16x32_bf16 v[72:75], a[176:179], v[152:155], v[72:75]\n"
    "v_cvt_pk_bf16_f32 v181, v210, v211\n"
    "v_mfma_f32_16x16x32_bf16 v[72:75], a[180:183], v[156:159], v[72:75]\n"
    "v_exp_f32 v212, v212\n"
    "v_mfma_f32_16x16x32_bf16 a[4:7], a[184:187], v[168:171], a[4:7]\n"
    "v_exp_f32 v213, v213\n"
    "v_mfma_f32_16x16x32_bf16 v[76:79], a[184:187], v[152:155], v[76:79]\n"
    "v_exp_f32 v214, v214\n"
    "v_mfma_f32_16x16x32_bf16 v[76:79], a[188:191], v[156:159], v[76:79]\n"
    "v_cvt_pk_bf16_f32 v182, v212, v213\n"
    "v_mfma_f32_16x16x32_bf16 v[216:219], a[112:115], a[64:67], 0\n"
    "v_exp_f32 v215, v215\n"
    "v_mfma_f32_16x16x32_bf16 v[216:219], a[116:119], a[68:71], v[216:219]\n"
    "v_cvt_pk_bf16_f32 v183, v214, v215\n"
    "v_mfma_f32_16x16x32_bf16 v[216:219], a[120:123], a[72:75], v[216:219]\n"
    "v_mfma_f32_16x16x32_bf16 v[216:219], a[124:127], a[76:79], v[216:219]\n"
    "v_mfma_f32_16x16x32_bf16 v[220:223], a[128:131], a[64:67], 0\n"
    "v_mfma_f32_16x16x32_bf16 v[220:223], a[132:135], a[68:71], v[220:223]\n"
    "v_mfma_f32_16x16x32_bf16 v[220:223], a[136:139], a[72:75], v[220:223]\n"
    "v_mfma_f32_16x16x32_bf16 v[220:223], a[140:143], a[76:79], v[220:223]\n"
    "v_exp_f32 v216, v216\n"
    "v_mfma_f32_16x16x32_bf16 a[216:219], a[228:231], v[152:155], a[216:219]\n"
    "v_exp_f32 v217, v217\n"
    "v_mfma_f32_16x16x32_bf16 a[216:219], a[228:231], v[156:159], a[216:219]\n"
    "v_exp_f32 v218, v218\n"
    "v_mfma_f32_16x16x32_bf16 v[104:107], a[176:179], v[160:163], v[104:107]\n"
    "v_cvt_pk_bf16_f32 v188, v216, v217\n"
    "v_mfma_f32_16x16x32_bf16 v[104:107], a[180:183], v[164:167], v[104:107]\n"
    "v_exp_f32 v219, v219\n"
    "v_mfma_f32_16x16x32_bf16 v[108:111], a[184:187], v[160:163], v[108:111]\n"
    "ds_read_b64_tr_b16 a[176:177], v4 offset:1024\n"
    "v_cvt_pk_bf16_f32 v189, v218, v219\n"
    "v_mfma_f32_16x16x32_bf16 v[108:111], a[188:191], v[164:167], v[108:111]\n"
    "v_exp_f32 v220, v220\n"
    "v_mfma_f32_16x16x32_bf16 a[224:227], a[228:231], v[168:171], a[224:227]\n"
    "v_exp_f32 v221, v221\n"
    "v_mfma_f32_16x16x32_bf16 a[220:223], a[228:231], v[160:163], a[220:223]\n"
    "v_exp_f32 v222, v222\n"
    "v_mfma_f32_16x16x32_bf16 a[220:223], a[228:231], v[164:167], a[220:223]\n"
    "ds_read_b64_tr_b16 a[178:179], v4 offset:5120\n"
    "v_cvt_pk_bf16_f32 v190, v220, v221\n"
    "v_mfma_f32_16x16x32_bf16 v[208:211], a[112:115], a[80:83], 0\n"
    "v_exp_f32 v223, v223\n"
    "v_mfma_f32_16x16x32_bf16 v[208:211], a[116:119], a[84:87], v[208:211]\n"
    "ds_read_b64_tr_b16 a[180:181], v6 offset:1024\n"
    "v_cvt_pk_bf16_f32 v191, v222, v223\n"
    "v_mfma_f32_16x16x32_bf16 v[208:211], a[120:123], a[88:91], v[208:211]\n"
    "ds_read_b64_tr_b16 a[182:183], v6 offset:5120\n"
    "ds_read_b64_tr_b16 a[184:185], v4 offset:1032\n"
    "v_mfma_f32_16x16x32_bf16 v[208:211], a[124:127], a[92:95], v[208:211]\n"
    "ds_read_b64_tr_b16 a[186:187], v4 offset:5128\n"
    "ds_read_b64_tr_b16 a[188:189], v6 offset:1032\n"
    "v_mfma_f32_16x16x32_bf16 v[212:215], a[128:131], a[80:83], 0\n"
    "ds_read_b64_tr_b16 a[190:191], v6 offset:5128\n"
    "v_mfma_f32_16x16x32_bf16 v[212:215], a[132:135], a[84:87], v[212:215]\n"
    "v_mfma_f32_16x16x32_bf16 v[212:215], a[136:139], a[88:91], v[212:215]\n"
    "v_mfma_f32_16x16x32_bf16 v[212:215], a[140:143], a[92:95], v[212:215]\n"
    "s_waitcnt lgkmcnt(1)\n"
    "v_exp_f32 v208, v208\n"
    "v_mfma_f32_16x16x32_bf16 v[16:19], a[192:195], v[136:139], v[16:19]\n"
    "v_exp_f32 v209, v209\n"
    "v_mfma_f32_16x16x32_bf16 v[16:19], a[196:199], v[140:143], v[16:19]\n"
    "v_exp_f32 v210, v210\n"
    "v_mfma_f32_16x16x32_bf16 v[20:23], a[200:203], v[136:139], v[20:23]\n"
    "v_cvt_pk_bf16_f32 v196, v208, v209\n"
    "v_mfma_f32_16x16x32_bf16 v[20:23], a[204:207], v[140:143], v[20:23]\n"
    "v_exp_f32 v211, v211\n"
    "v_mfma_f32_16x16x32_bf16 v[48:51], a[192:195], v[144:147], v[48:51]\n"
    "v_cvt_pk_bf16_f32 v197, v210, v211\n"
    "v_mfma_f32_16x16x32_bf16 v[48:51], a[196:199], v[148:151], v[48:51]\n"
    "v_exp_f32 v212, v212\n"
    "v_mfma_f32_16x16x32_bf16 a[8:11], a[192:195], v[168:171], a[8:11]\n"
    "v_exp_f32 v213, v213\n"
    "v_mfma_f32_16x16x32_bf16 v[52:55], a[200:203], v[144:147], v[52:55]\n"
    "v_exp_f32 v214, v214\n"
    "v_mfma_f32_16x16x32_bf16 v[52:55], a[204:207], v[148:151], v[52:55]\n"
    "v_cvt_pk_bf16_f32 v198, v212, v213\n"
    "v_mfma_f32_16x16x32_bf16 v[216:219], a[112:115], a[96:99], 0\n"
    "v_exp_f32 v215, v215\n"
    "v_mfma_f32_16x16x32_bf16 v[216:219], a[116:119], a[100:103], v[216:219]\n"
    "v_cvt_pk_bf16_f32 v199, v214, v215\n"
    "v_mfma_f32_16x16x32_bf16 v[216:219], a[120:123], a[104:107], v[216:219]\n"
    "v_mfma_f32_16x16x32_bf16 v[216:219], a[124:127], a[108:111], v[216:219]\n"
    "v_mfma_f32_16x16x32_bf16 v[220:223], a[128:131], a[96:99], 0\n"
    "v_mfma_f32_16x16x32_bf16 v[220:223], a[132:135], a[100:103], v[220:223]\n"
    "v_mfma_f32_16x16x32_bf16 v[220:223], a[136:139], a[104:107], v[220:223]\n"
    "v_mfma_f32_16x16x32_bf16 v[220:223], a[140:143], a[108:111], v[220:223]\n"
    "v_exp_f32 v216, v216\n"
    "v_mfma_f32_16x16x32_bf16 v[80:83], a[192:195], v[152:155], v[80:83]\n"
    "v_exp_f32 v217, v217\n"
    "v_mfma_f32_16x16x32_bf16 v[80:83], a[196:199], v[156:159], v[80:83]\n"
    "v_exp_f32 v218, v218\n"
    "v_mfma_f32_16x16x32_bf16 v[84:87], a[200:203], v[152:155], v[84:87]\n"
    "v_cvt_pk_bf16_f32 v204, v216, v217\n"
    "v_mfma_f32_16x16x32_bf16 v[84:87], a[204:207], v[156:159], v[84:87]\n"
    "v_exp_f32 v219, v219\n"
    "v_mfma_f32_16x16x32_bf16 v[112:115], a[192:195], v[160:163], v[112:115]\n"
    "v_cvt_pk_bf16_f32 v205, v218, v219\n"
    "v_mfma_f32_16x16x32_bf16 v[112:115], a[196:199], v[164:167], v[112:115]\n"
    "v_exp_f32 v220, v220\n"
    "v_mfma_f32_16x16x32_bf16 a[12:15], a[200:203], v[168:171], a[12:15]\n"
    "v_exp_f32 v221, v221\n"
    "v_mfma_f32_16x16x32_bf16 v[116:119], a[200:203], v[160:163], v[116:119]\n"
    "v_exp_f32 v222, v222\n"
    "v_mfma_f32_16x16x32_bf16 v[116:119], a[204:207], v[164:167], v[116:119]\n"
    "s_waitcnt vmcnt(8)\n"
    "s_barrier\n"
    "ds_read_b64_tr_b16 a[192:193], v5 offset:1024\n"
    "v_cvt_pk_bf16_f32 v206, v220, v221\n"
    "v_mfma_f32_16x16x32_bf16 v[208:211], a[144:147], a[32:35], 0\n"
    "s_add_u32 m0, s70, 0\n"
    "v_exp_f32 v223, v223\n"
    "v_mfma_f32_16x16x32_bf16 v[208:211], a[148:151], a[36:39], v[208:211]\n"
    "ds_read_b64_tr_b16 a[194:195], v5 offset:5120\n"
    "v_cvt_pk_bf16_f32 v207, v222, v223\n"
    "v_mfma_f32_16x16x32_bf16 v[208:211], a[152:155], a[40:43], v[208:211]\n"
    "ds_read_b64_tr_b16 a[196:197], v7 offset:1024\n"
    "ds_read_b64_tr_b16 a[198:199], v7 offset:5120\n"
    "v_mfma_f32_16x16x32_bf16 v[208:211], a[156:159], a[44:47], v[208:211]\n"
    "ds_read_b64_tr_b16 a[200:201], v5 offset:1032\n"
    "ds_read_b64_tr_b16 a[202:203], v5 offset:5128\n"
    "v_mfma_f32_16x16x32_bf16 v[212:215], a[160:163], a[32:35], 0\n"
    "buffer_load_dwordx4 v236, s[28:31], s73 offen sc1 lds\n"
    "v_mfma_f32_16x16x32_bf16 v[212:215], a[164:167], a[36:39], v[212:215]\n"
    "ds_read_b64_tr_b16 a[204:205], v7 offset:1032\n"
    "ds_read_b64_tr_b16 a[206:207], v7 offset:5128\n"
    "v_mfma_f32_16x16x32_bf16 v[212:215], a[168:171], a[40:43], v[212:215]\n"
    "ds_read_b128 a[112:115], v224 offset:0\n"
    "ds_read_b128 a[116:119], v225 offset:0\n"
    "v_mfma_f32_16x16x32_bf16 v[212:215], a[172:175], a[44:47], v[212:215]\n"
    "v_exp_f32 v208, v208\n"
    "v_mfma_f32_16x16x32_bf16 v[24:27], a[176:179], v[136:139], v[24:27]\n"
    "v_exp_f32 v209, v209\n"
    "v_mfma_f32_16x16x32_bf16 v[24:27], a[180:183], v[140:143], v[24:27]\n"
    "buffer_load_dwordx4 v236, s[28:31], s73 offen offset:2048 sc1 lds\n"
    "v_mfma_f32_16x16x32_bf16 v[28:31], a[184:187], v[136:139], v[28:31]\n"
    "s_waitcnt lgkmcnt(0)\n"
    "v_exp_f32 v210, v210\n"
    "v_mfma_f32_16x16x32_bf16 v[28:31], a[188:191], v[140:143], v[28:31]\n"
    "ds_read_b128 a[120:123], v224 offset:1024\n"
    "v_cvt_pk_bf16_f32 v176, v208, v209\n"
    "v_mfma_f32_16x16x32_bf16 v[56:59], a[176:179], v[144:147], v[56:59]\n"
    "v_exp_f32 v211, v211\n"
    "v_mfma_f32_16x16x32_bf16 v[56:59], a[180:183], v[148:151], v[56:59]\n"
    "ds_read_b128 a[124:127], v225 offset:1024\n"
    "v_cvt_pk_bf16_f32 v177, v210, v211\n"
    "v_mfma_f32_16x16x32_bf16 a[16:19], a[176:179], v[168:171], a[16:19]\n"
    "s_add_u32 m0, s70, 1024\n"
    "v_exp_f32 v212, v212\n"
    "v_mfma_f32_16x16x32_bf16 v[60:63], a[184:187], v[144:147], v[60:63]\n"
    "v_exp_f32 v213, v213\n"
    "v_mfma_f32_16x16x32_bf16 v[60:63], a[188:191], v[148:151], v[60:63]\n"
    "v_exp_f32 v214, v214\n"
    "v_mfma_f32_16x16x32_bf16 v[216:219], a[144:147], a[48:51], 0\n"
    "ds_read_b128 a[128:131], v224 offset:4096\n"
    "v_cvt_pk_bf16_f32 v178, v212, v213\n"
    "v_mfma_f32_16x16x32_bf16 v[216:219], a[148:151], a[52:55], v[216:219]\n"
    "v_exp_f32 v215, v215\n"
    "v_mfma_f32_16x16x32_bf16 v[216:219], a[152:155], a[56:59], v[216:219]\n"
    "buffer_load_dwordx4 v237, s[28:31], s73 offen sc1 lds\n"
    "v_mfma_f32_16x16x32_bf16 v[216:219], a[156:159], a[60:63], v[216:219]\n"
    "ds_read_b128 a[132:135], v225 offset:4096\n"
    "v_cvt_pk_bf16_f32 v179, v214, v215\n"
    "v_mfma_f32_16x16x32_bf16 v[220:223], a[160:163], a[48:51], 0\n"
    "ds_read_b128 a[136:139], v224 offset:5120\n"
    "ds_read_b128 a[140:143], v225 offset:5120\n"
    "v_mfma_f32_16x16x32_bf16 v[220:223], a[164:167], a[52:55], v[220:223]\n"
    "v_mfma_f32_16x16x32_bf16 v[220:223], a[168:171], a[56:59], v[220:223]\n"
    "v_mfma_f32_16x16x32_bf16 v[220:223], a[172:175], a[60:63], v[220:223]\n"
    "buffer_load_dwordx4 v237, s[28:31], s73 offen offset:2048 sc1 lds\n"
    "v_mfma_f32_16x16x32_bf16 v[88:91], a[176:179], v[152:155], v[88:91]\n"
    "v_exp_f32 v216, v216\n"
    "v_mfma_f32_16x16x32_bf16 v[88:91], a[180:183], v[156:159], v[88:91]\n"
    "v_exp_f32 v217, v217\n"
    "v_mfma_f32_16x16x32_bf16 v[92:95], a[184:187], v[152:155], v[92:95]\n"
    "v_exp_f32 v218, v218\n"
    "v_mfma_f32_16x16x32_bf16 v[92:95], a[188:191], v[156:159], v[92:95]\n"
    "v_cvt_pk_bf16_f32 v184, v216, v217\n"
    "v_mfma_f32_16x16x32_bf16 v[120:123], a[176:179], v[160:163], v[120:123]\n"
    "s_add_u32 m0, s71, 0\n"
    "v_exp_f32 v219, v219\n"
    "v_mfma_f32_16x16x32_bf16 v[120:123], a[180:183], v[164:167], v[120:123]\n"
    "ds_read_b64_tr_b16 a[176:177], v4 offset:16384\n"
    "v_cvt_pk_bf16_f32 v185, v218, v219\n"
    "v_mfma_f32_16x16x32_bf16 a[20:23], a[184:187], v[168:171], a[20:23]\n"
    "v_exp_f32 v220, v220\n"
    "v_mfma_f32_16x16x32_bf16 v[124:127], a[184:187], v[160:163], v[124:127]\n"
    "v_exp_f32 v221, v221\n"
    "v_mfma_f32_16x16x32_bf16 v[124:127], a[188:191], v[164:167], v[124:127]\n"
    "buffer_load_dwordx4 v236, s[24:27], s74 offen sc1 lds\n"
    "v_mfma_f32_16x16x32_bf16 v[208:211], a[144:147], a[64:67], 0\n"
    "v_exp_f32 v222, v222\n"
    "v_mfma_f32_16x16x32_bf16 v[208:211], a[148:151], a[68:71], v[208:211]\n"
    "ds_read_b64_tr_b16 a[178:179], v4 offset:20480\n"
    "v_cvt_pk_bf16_f32 v186, v220, v221\n"
    "v_mfma_f32_16x16x32_bf16 v[208:211], a[152:155], a[72:75], v[208:211]\n"
    "v_exp_f32 v223, v223\n"
    "v_mfma_f32_16x16x32_bf16 v[208:211], a[156:159], a[76:79], v[208:211]\n"
    "ds_read_b64_tr_b16 a[180:181], v6 offset:16384\n"
    "v_cvt_pk_bf16_f32 v187, v222, v223\n"
    "v_mfma_f32_16x16x32_bf16 v[212:215], a[160:163], a[64:67], 0\n"
    "buffer_load_dwordx4 v236, s[24:27], s74 offen offset:2048 sc1 lds\n"
    "v_mfma_f32_16x16x32_bf16 v[212:215], a[164:167], a[68:71], v[212:215]\n"
    "ds_read_b64_tr_b16 a[182:183], v6 offset:20480\n"
    "ds_read_b64_tr_b16 a[184:185], v4 offset:16392\n"
    "v_mfma_f32_16x16x32_bf16 v[212:215], a[168:171], a[72:75], v[212:215]\n"
    "ds_read_b64_tr_b16 a[186:187], v4 offset:20488\n"
    "ds_read_b64_tr_b16 a[188:189], v6 offset:16392\n"
    "v_mfma_f32_16x16x32_bf16 v[212:215], a[172:175], a[76:79], v[212:215]\n"
    "v_exp_f32 v208, v208\n"
    "v_mfma_f32_16x16x32_bf16 v[32:35], a[192:195], v[136:139], v[32:35]\n"
    "v_exp_f32 v209, v209\n"
    "v_mfma_f32_16x16x32_bf16 v[32:35], a[196:199], v[140:143], v[32:35]\n"
    "s_add_u32 m0, s71, 1024\n"
    "v_exp_f32 v210, v210\n"
    "v_mfma_f32_16x16x32_bf16 v[36:39], a[200:203], v[136:139], v[36:39]\n"
    "ds_read_b64_tr_b16 a[190:191], v6 offset:20488\n"
    "v_cvt_pk_bf16_f32 v192, v208, v209\n"
    "v_mfma_f32_16x16x32_bf16 v[36:39], a[204:207], v[140:143], v[36:39]\n"
    "v_exp_f32 v211, v211\n"
    "v_mfma_f32_16x16x32_bf16 v[64:67], a[192:195], v[144:147], v[64:67]\n"
    "v_cvt_pk_bf16_f32 v193, v210, v211\n"
    "v_mfma_f32_16x16x32_bf16 v[64:67], a[196:199], v[148:151], v[64:67]\n"
    "v_exp_f32 v212, v212\n"
    "v_mfma_f32_16x16x32_bf16 a[24:27], a[192:195], v[168:171], a[24:27]\n"
    "buffer_load_dwordx4 v237, s[24:27], s74 offen sc1 lds\n"
    "v_mfma_f32_16x16x32_bf16 v[68:71], a[200:203], v[144:147], v[68:71]\n"
    "v_exp_f32 v213, v213\n"
    "v_mfma_f32_16x16x32_bf16 v[68:71], a[204:207], v[148:151], v[68:71]\n"
    "v_exp_f32 v214, v214\n"
    "v_mfma_f32_16x16x32_bf16 v[216:219], a[144:147], a[80:83], 0\n"
    "v_cvt_pk_bf16_f32 v194, v212, v213\n"
    "v_mfma_f32_16x16x32_bf16 v[216:219], a[148:151], a[84:87], v[216:219]\n"
    "v_exp_f32 v215, v215\n"
    "v_mfma_f32_16x16x32_bf16 v[216:219], a[152:155], a[88:91], v[216:219]\n"
    "buffer_load_dwordx4 v237, s[24:27], s74 offen offset:2048 sc1 lds\n"
    "v_mfma_f32_16x16x32_bf16 v[216:219], a[156:159], a[92:95], v[216:219]\n"
    "ds_read_b128 a[144:147], v226 offset:0\n"
    "v_cvt_pk_bf16_f32 v195, v214, v215\n"
    "v_mfma_f32_16x16x32_bf16 v[220:223], a[160:163], a[80:83], 0\n"
    "ds_read_b128 a[148:151], v227 offset:0\n"
    "ds_read_b128 a[152:155], v226 offset:1024\n"
    "v_mfma_f32_16x16x32_bf16 v[220:223], a[164:167], a[84:87], v[220:223]\n"
    "ds_read_b128 a[156:159], v227 offset:1024\n"
    "ds_read_b128 a[160:163], v226 offset:4096\n"
    "v_mfma_f32_16x16x32_bf16 v[220:223], a[168:171], a[88:91], v[220:223]\n"
    "ds_read_b128 a[164:167], v227 offset:4096\n"
    "v_mfma_f32_16x16x32_bf16 v[220:223], a[172:175], a[92:95], v[220:223]\n"
    "v_exp_f32 v216, v216\n"
    "v_mfma_f32_16x16x32_bf16 v[96:99], a[192:195], v[152:155], v[96:99]\n"
    "v_exp_f32 v217, v217\n"
    "v_mfma_f32_16x16x32_bf16 v[96:99], a[196:199], v[156:159], v[96:99]\n"
    "v_exp_f32 v218, v218\n"
    "v_mfma_f32_16x16x32_bf16 v[100:103], a[200:203], v[152:155], v[100:103]\n"
    "ds_read_b128 a[168:171], v226 offset:5120\n"
    "v_cvt_pk_bf16_f32 v200, v216, v217\n"
    "v_mfma_f32_16x16x32_bf16 v[100:103], a[204:207], v[156:159], v[100:103]\n"
    "v_exp_f32 v219, v219\n"
    "v_mfma_f32_16x16x32_bf16 v[128:131], a[192:195], v[160:163], v[128:131]\n"
    "ds_read_b128 a[172:175], v227 offset:5120\n"
    "v_cvt_pk_bf16_f32 v201, v218, v219\n"
    "v_mfma_f32_16x16x32_bf16 v[128:131], a[196:199], v[164:167], v[128:131]\n"
    "v_exp_f32 v220, v220\n"
    "v_mfma_f32_16x16x32_bf16 a[28:31], a[200:203], v[168:171], a[28:31]\n"
    "v_exp_f32 v221, v221\n"
    "v_mfma_f32_16x16x32_bf16 v[132:135], a[200:203], v[160:163], v[132:135]\n"
    "v_exp_f32 v222, v222\n"
    "v_mfma_f32_16x16x32_bf16 v[132:135], a[204:207], v[164:167], v[132:135]\n"
    "ds_read_b64_tr_b16 a[192:193], v5 offset:16384\n"
    "v_cvt_pk_bf16_f32 v202, v220, v221\n"
    "v_swap_b32 v224, v232\n"
    "v_swap_b32 v225, v233\n"
    "v_swap_b32 v226, v234\n"
    "v_swap_b32 v227, v235\n"
    "v_swap_b32 v228, v4\n"
    "v_swap_b32 v229, v5\n"
    "v_swap_b32 v230, v6\n"
    "v_swap_b32 v231, v7\n"
    "s_xor_b32 s68, s68, 0x8000\n"
    "s_xor_b32 s69, s69, 0x8000\n"
    "s_xor_b32 s70, s70, 0x8000\n"
    "s_xor_b32 s71, s71, 0x8000\n"
    "s_add_u32 s72, s72, 32768\n"
    "s_add_u32 s73, s73, 32768\n"
    "s_add_u32 s74, s74, 32768\n"
    "s_sub_u32 s75, s75, 1\n"
    "s_branch 1b\n"
    "4:\n"
    "v_mfma_f32_16x16x32_bf16 v[216:219], a[112:115], a[32:35], 0\n"
    "ds_read_b64_tr_b16 a[194:195], v5 offset:4096\n"
    "ds_read_b64_tr_b16 a[196:197], v7 offset:0\n"
    "v_mfma_f32_16x16x32_bf16 v[216:219], a[116:119], a[36:39], v[216:219]\n"
    "s_waitcnt lgkmcnt(3)\n"
    "ds_read_b64_tr_b16 a[198:199], v7 offset:4096\n"
    "ds_read_b64_tr_b16 a[200:201], v5 offset:8\n"
    "v_mfma_f32_16x16x32_bf16 v[216:219], a[120:123], a[40:43], v[216:219]\n"
    "ds_read_b64_tr_b16 a[202:203], v5 offset:4104\n"
    "ds_read_b64_tr_b16 a[204:205], v7 offset:8\n"
    "v_mfma_f32_16x16x32_bf16 v[216:219], a[124:127], a[44:47], v[216:219]\n"
    "ds_read_b64_tr_b16 a[206:207], v7 offset:4104\n"
    "v_mov_b32 v2, s62\n"
    "v_mfma_f32_16x16x32_bf16 v[220:223], a[128:131], a[32:35], 0\n"
    "v_exp_f32 v215, v215\n"
    "v_mfma_f32_16x16x32_bf16 v[220:223], a[132:135], a[36:39], v[220:223]\n"
    "v_cvt_pk_bf16_f32 v167, v214, v215\n"
    "v_mov_b32 v3, s63\n"
    "v_mfma_f32_16x16x32_bf16 v[220:223], a[136:139], a[40:43], v[220:223]\n"
    "v_mfma_f32_16x16x32_bf16 v[220:223], a[140:143], a[44:47], v[220:223]\n"
    "v_exp_f32 v216, v216\n"
    "v_mfma_f32_16x16x32_bf16 v[8:11], a[176:179], v[136:139], v[8:11]\n"
    "v_exp_f32 v217, v217\n"
    "v_mfma_f32_16x16x32_bf16 v[8:11], a[180:183], v[140:143], v[8:11]\n"
    "v_exp_f32 v218, v218\n"
    "v_mfma_f32_16x16x32_bf16 v[12:15], a[184:187], v[136:139], v[12:15]\n"
    "v_cvt_pk_bf16_f32 v172, v216, v217\n"
    "v_mfma_f32_16x16x32_bf16 v[12:15], a[188:191], v[140:143], v[12:15]\n"
    "v_exp_f32 v219, v219\n"
    "v_mfma_f32_16x16x32_bf16 a[208:211], a[228:231], v[136:139], a[208:211]\n"
    "v_cvt_pk_bf16_f32 v173, v218, v219\n"
    "v_mfma_f32_16x16x32_bf16 a[208:211], a[228:231], v[140:143], a[208:211]\n"
    "v_exp_f32 v220, v220\n"
    "v_mfma_f32_16x16x32_bf16 v[40:43], a[176:179], v[144:147], v[40:43]\n"
    "v_exp_f32 v221, v221\n"
    "v_mfma_f32_16x16x32_bf16 v[40:43], a[180:183], v[148:151], v[40:43]\n"
    "ds_read_b64_tr_b16 a[176:177], v4 offset:1024\n"
    "ds_read_b64_tr_b16 a[178:179], v4 offset:5120\n"
    "v_mfma_f32_16x16x32_bf16 v[44:47], a[184:187], v[144:147], v[44:47]\n"
    "ds_read_b64_tr_b16 a[180:181], v6 offset:1024\n"
    "ds_read_b64_tr_b16 a[182:183], v6 offset:5120\n"
    "v_mfma_f32_16x16x32_bf16 v[44:47], a[188:191], v[148:151], v[44:47]\n"
    "ds_read_b64_tr_b16 a[184:185], v4 offset:1032\n"
    "ds_read_b64_tr_b16 a[186:187], v4 offset:5128\n"
    "v_mfma_f32_16x16x32_bf16 v[208:211], a[112:115], a[48:51], 0\n"
    "v_exp_f32 v222, v222\n"
    "v_mfma_f32_16x16x32_bf16 v[208:211], a[116:119], a[52:55], v[208:211]\n"
    "ds_read_b64_tr_b16 a[188:189], v6 offset:1032\n"
    "v_cvt_pk_bf16_f32 v174, v220, v221\n"
    "v_mfma_f32_16x16x32_bf16 v[208:211], a[120:123], a[56:59], v[208:211]\n"
    "ds_read_b64_tr_b16 a[190:191], v6 offset:5128\n"
    "v_mfma_f32_16x16x32_bf16 v[208:211], a[124:127], a[60:63], v[208:211]\n"
    "v_exp_f32 v223, v223\n"
    "v_mfma_f32_16x16x32_bf16 v[212:215], a[128:131], a[48:51], 0\n"
    "v_cvt_pk_bf16_f32 v175, v222, v223\n"
    "v_mfma_f32_16x16x32_bf16 v[212:215], a[132:135], a[52:55], v[212:215]\n"
    "v_mfma_f32_16x16x32_bf16 v[212:215], a[136:139], a[56:59], v[212:215]\n"
    "v_mfma_f32_16x16x32_bf16 v[212:215], a[140:143], a[60:63], v[212:215]\n"
    "v_exp_f32 v208, v208\n"
    "v_mfma_f32_16x16x32_bf16 a[212:215], a[228:231], v[144:147], a[212:215]\n"
    "v_exp_f32 v209, v209\n"
    "v_mfma_f32_16x16x32_bf16 a[212:215], a[228:231], v[148:151], a[212:215]\n"
    "s_waitcnt lgkmcnt(0)\n"
    "v_exp_f32 v210, v210\n"
    "v_mfma_f32_16x16x32_bf16 v[16:19], a[192:195], v[136:139], v[16:19]\n"
    "v_cvt_pk_bf16_f32 v180, v208, v209\n"
    "v_mfma_f32_16x16x32_bf16 v[16:19], a[196:199], v[140:143], v[16:19]\n"
    "v_exp_f32 v211, v211\n"
    "v_mfma_f32_16x16x32_bf16 v[20:23], a[200:203], v[136:139], v[20:23]\n"
    "v_cvt_pk_bf16_f32 v181, v210, v211\n"
    "v_mfma_f32_16x16x32_bf16 v[20:23], a[204:207], v[140:143], v[20:23]\n"
    "v_exp_f32 v212, v212\n"
    "v_mfma_f32_16x16x32_bf16 v[48:51], a[192:195], v[144:147], v[48:51]\n"
    "v_exp_f32 v213, v213\n"
    "v_mfma_f32_16x16x32_bf16 v[48:51], a[196:199], v[148:151], v[48:51]\n"
    "s_waitcnt vmcnt(12)\n"
    "s_barrier\n"
    "ds_read_b64_tr_b16 a[192:193], v5 offset:1024\n"
    "ds_read_b64_tr_b16 a[194:195], v5 offset:5120\n"
    "v_mfma_f32_16x16x32_bf16 v[216:219], a[144:147], a[32:35], 0\n"
    "ds_read_b64_tr_b16 a[196:197], v7 offset:1024\n"
    "ds_read_b64_tr_b16 a[198:199], v7 offset:5120\n"
    "v_mfma_f32_16x16x32_bf16 v[216:219], a[148:151], a[36:39], v[216:219]\n"
    "v_exp_f32 v214, v214\n"
    "v_mfma_f32_16x16x32_bf16 v[216:219], a[152:155], a[40:43], v[216:219]\n"
    "ds_read_b64_tr_b16 a[232:233], v6 offset:16384\n"
    "v_cvt_pk_bf16_f32 v182, v212, v213\n"
    "v_mfma_f32_16x16x32_bf16 v[216:219], a[156:159], a[44:47], v[216:219]\n"
    "v_exp_f32 v215, v215\n"
    "v_mfma_f32_16x16x32_bf16 v[220:223], a[160:163], a[32:35], 0\n"
    "ds_read_b64_tr_b16 a[234:235], v6 offset:20480\n"
    "v_cvt_pk_bf16_f32 v183, v214, v215\n"
    "v_mfma_f32_16x16x32_bf16 v[220:223], a[164:167], a[36:39], v[220:223]\n"
    "ds_read_b64_tr_b16 a[236:237], v6 offset:16392\n"
    "ds_read_b64_tr_b16 a[238:239], v6 offset:20488\n"
    "v_mfma_f32_16x16x32_bf16 v[220:223], a[168:171], a[40:43], v[220:223]\n"
    "ds_read_b64_tr_b16 a[240:241], v7 offset:16384\n"
    "ds_read_b64_tr_b16 a[242:243], v7 offset:20480\n"
    "v_mfma_f32_16x16x32_bf16 v[220:223], a[172:175], a[44:47], v[220:223]\n"
    "v_exp_f32 v216, v216\n"
    "v_mfma_f32_16x16x32_bf16 v[52:55], a[200:203], v[144:147], v[52:55]\n"
    "v_exp_f32 v217, v217\n"
    "v_mfma_f32_16x16x32_bf16 v[52:55], a[204:207], v[148:151], v[52:55]\n"
    "ds_read_b64_tr_b16 a[200:201], v5 offset:1032\n"
    "ds_read_b64_tr_b16 a[202:203], v5 offset:5128\n"
    "v_mfma_f32_16x16x32_bf16 v[24:27], a[176:179], v[136:139], v[24:27]\n"
    "ds_read_b64_tr_b16 a[204:205], v7 offset:1032\n"
    "ds_read_b64_tr_b16 a[206:207], v7 offset:5128\n"
    "v_mfma_f32_16x16x32_bf16 v[24:27], a[180:183], v[140:143], v[24:27]\n"
    "v_exp_f32 v218, v218\n"
    "v_mfma_f32_16x16x32_bf16 v[28:31], a[184:187], v[136:139], v[28:31]\n"
    "ds_read_b64_tr_b16 a[244:245], v7 offset:16392\n"
    "v_cvt_pk_bf16_f32 v176, v216, v217\n"
    "v_mfma_f32_16x16x32_bf16 v[28:31], a[188:191], v[140:143], v[28:31]\n"
    "v_exp_f32 v219, v219\n"
    "v_mfma_f32_16x16x32_bf16 v[56:59], a[176:179], v[144:147], v[56:59]\n"
    "ds_read_b64_tr_b16 a[246:247], v7 offset:20488\n"
    "v_cvt_pk_bf16_f32 v177, v218, v219\n"
    "v_mfma_f32_16x16x32_bf16 v[56:59], a[180:183], v[148:151], v[56:59]\n"
    "ds_read_b64_tr_b16 a[176:177], v4 offset:16384\n"
    "ds_read_b64_tr_b16 a[178:179], v4 offset:20480\n"
    "v_mfma_f32_16x16x32_bf16 v[60:63], a[184:187], v[144:147], v[60:63]\n"
    "ds_read_b64_tr_b16 a[180:181], v4 offset:16392\n"
    "ds_read_b64_tr_b16 a[182:183], v4 offset:20488\n"
    "v_mfma_f32_16x16x32_bf16 v[60:63], a[188:191], v[148:151], v[60:63]\n"
    "ds_read_b64_tr_b16 a[184:185], v5 offset:16384\n"
    "ds_read_b64_tr_b16 a[186:187], v5 offset:20480\n"
    "v_mfma_f32_16x16x32_bf16 v[208:211], a[144:147], a[48:51], 0\n"
    "v_exp_f32 v220, v220\n"
    "v_mfma_f32_16x16x32_bf16 v[208:211], a[148:151], a[52:55], v[208:211]\n"
    "v_exp_f32 v221, v221\n"
    "v_mfma_f32_16x16x32_bf16 v[208:211], a[152:155], a[56:59], v[208:211]\n"
    "ds_read_b64_tr_b16 a[188:189], v5 offset:16392\n"
    "ds_read_b64_tr_b16 a[190:191], v5 offset:20488\n"
    "v_mfma_f32_16x16x32_bf16 v[208:211], a[156:159], a[60:63], v[208:211]\n"
    "v_exp_f32 v222, v222\n"
    "v_mfma_f32_16x16x32_bf16 v[212:215], a[160:163], a[48:51], 0\n"
    "ds_read_b64_tr_b16 a[248:249], v6 offset:17408\n"
    "v_cvt_pk_bf16_f32 v178, v220, v221\n"
    "v_mfma_f32_16x16x32_bf16 v[212:215], a[164:167], a[52:55], v[212:215]\n"
    "v_exp_f32 v223, v223\n"
    "v_mfma_f32_16x16x32_bf16 v[212:215], a[168:171], a[56:59], v[212:215]\n"
    "ds_read_b64_tr_b16 a[250:251], v6 offset:21504\n"
    "v_cvt_pk_bf16_f32 v179, v222, v223\n"
    "v_mfma_f32_16x16x32_bf16 v[212:215], a[172:175], a[60:63], v[212:215]\n"
    "s_waitcnt lgkmcnt(2)\n"
    "ds_read_b64_tr_b16 a[252:253], v6 offset:17416\n"
    "ds_read_b64_tr_b16 a[254:255], v6 offset:21512\n"
    "v_mfma_f32_16x16x32_bf16 v[32:35], a[192:195], v[136:139], v[32:35]\n"
    "ds_read_b64_tr_b16 a[32:33], v7 offset:17408\n"
    "ds_read_b64_tr_b16 a[34:35], v7 offset:21504\n"
    "v_mfma_f32_16x16x32_bf16 v[32:35], a[196:199], v[140:143], v[32:35]\n"
    "ds_read_b64_tr_b16 a[36:37], v7 offset:17416\n"
    "ds_read_b64_tr_b16 a[38:39], v7 offset:21512\n"
    "v_mfma_f32_16x16x32_bf16 v[36:39], a[200:203], v[136:139], v[36:39]\n"
    "v_exp_f32 v208, v208\n"
    "v_mfma_f32_16x16x32_bf16 v[36:39], a[204:207], v[140:143], v[36:39]\n"
    "v_exp_f32 v209, v209\n"
    "v_mfma_f32_16x16x32_bf16 v[64:67], a[192:195], v[144:147], v[64:67]\n"
    "v_exp_f32 v210, v210\n"
    "v_mfma_f32_16x16x32_bf16 v[64:67], a[196:199], v[148:151], v[64:67]\n"
    "ds_read_b64_tr_b16 a[192:193], v4 offset:17408\n"
    "ds_read_b64_tr_b16 a[194:195], v4 offset:21504\n"
    "v_mfma_f32_16x16x32_bf16 v[68:71], a[200:203], v[144:147], v[68:71]\n"
    "ds_read_b64_tr_b16 a[196:197], v4 offset:17416\n"
    "ds_read_b64_tr_b16 a[198:199], v4 offset:21512\n"
    "v_mfma_f32_16x16x32_bf16 v[68:71], a[204:207], v[148:151], v[68:71]\n"
    "ds_read_b64_tr_b16 a[200:201], v5 offset:17408\n"
    "ds_read_b64_tr_b16 a[202:203], v5 offset:21504\n"
    "v_mfma_f32_16x16x32_bf16 a[208:211], a[228:231], v[172:175], a[208:211]\n"
    "ds_read_b64_tr_b16 a[204:205], v5 offset:17416\n"
    "ds_read_b64_tr_b16 a[206:207], v5 offset:21512\n"
    "v_mfma_f32_16x16x32_bf16 a[208:211], a[228:231], v[176:179], a[208:211]\n"
    "v_cvt_pk_bf16_f32 v184, v208, v209\n"
    "v_mfma_f32_16x16x32_bf16 v[8:11], a[176:179], v[172:175], v[8:11]\n"
    "v_exp_f32 v211, v211\n"
    "v_mfma_f32_16x16x32_bf16 v[8:11], a[232:235], v[176:179], v[8:11]\n"
    "v_cvt_pk_bf16_f32 v185, v210, v211\n"
    "v_mfma_f32_16x16x32_bf16 v[12:15], a[180:183], v[172:175], v[12:15]\n"
    "v_exp_f32 v212, v212\n"
    "v_mfma_f32_16x16x32_bf16 v[12:15], a[236:239], v[176:179], v[12:15]\n"
    "v_accvgpr_read_b32 v255, a208\n"
    "v_mfma_f32_16x16x32_bf16 v[16:19], a[184:187], v[172:175], v[16:19]\n"
    "v_rcp_f32 v248, v255\n"
    "v_mfma_f32_16x16x32_bf16 v[16:19], a[240:243], v[176:179], v[16:19]\n"
    "v_cmp_nge_f32 vcc, v255, v246\n"
    "v_mul_f32 v8, v8, v248\n"
    "v_mfma_f32_16x16x32_bf16 v[20:23], a[188:191], v[172:175], v[20:23]\n"
    "s_or_b64 s[52:53], s[52:53], vcc\n"
    "v_mul_f32 v9, v9, v248\n"
    "v_mul_f32 v10, v10, v248\n"
    "v_mfma_f32_16x16x32_bf16 v[20:23], a[244:247], v[176:179], v[20:23]\n"
    "s_waitcnt lgkmcnt(0)\n"
    "v_cmp_nle_f32 vcc, v255, v247\n"
    "v_mul_f32 v11, v11, v248\n"
    "v_mfma_f32_16x16x32_bf16 v[24:27], a[192:195], v[172:175], v[24:27]\n"
    "s_or_b64 s[52:53], s[52:53], vcc\n"
    "v_mul_f32 v12, v12, v248\n"
    "v_mul_f32 v13, v13, v248\n"
    "v_mfma_f32_16x16x32_bf16 v[24:27], a[248:251], v[176:179], v[24:27]\n"
    "v_mul_f32 v14, v14, v248\n"
    "v_mul_f32 v15, v15, v248\n"
    "v_mfma_f32_16x16x32_bf16 v[28:31], a[196:199], v[172:175], v[28:31]\n"
    "v_cvt_pk_bf16_f32 v8, v8, v9\n"
    "v_cvt_pk_bf16_f32 v9, v10, v11\n"
    "v_mfma_f32_16x16x32_bf16 v[28:31], a[252:255], v[176:179], v[28:31]\n"
    "v_cvt_pk_bf16_f32 v10, v12, v13\n"
    "v_cvt_pk_bf16_f32 v11, v14, v15\n"
    "v_mfma_f32_16x16x32_bf16 v[32:35], a[200:203], v[172:175], v[32:35]\n"
    "v_exp_f32 v213, v213\n"
    "v_mfma_f32_16x16x32_bf16 v[32:35], a[32:35], v[176:179], v[32:35]\n"
    "v_exp_f32 v214, v214\n"
    "v_mfma_f32_16x16x32_bf16 v[36:39], a[204:207], v[172:175], v[36:39]\n"
    "v_cvt_pk_bf16_f32 v186, v212, v213\n"
    "v_mul_f32 v16, v16, v248\n"
    "v_mfma_f32_16x16x32_bf16 v[36:39], a[36:39], v[176:179], v[36:39]\n"
    "buffer_store_dwordx4 v[8:11], v240, s[64:67], s48 offen offset:0 sc1\n"
    "v_mfma_f32_16x16x32_bf16 a[212:215], a[228:231], v[180:183], a[212:215]\n"
    "v_exp_f32 v215, v215\n"
    "s_nop 0\n"
    "v_cvt_pk_bf16_f32 v187, v214, v215\n"
    "v_mul_f32 v17, v17, v248\n"
    "v_mul_f32 v18, v18, v248\n"
    "v_mul_f32 v19, v19, v248\n"
    "v_mfma_f32_16x16x32_bf16 a[212:215], a[228:231], v[184:187], a[212:215]\n"
    "v_mul_f32 v20, v20, v248\n"
    "v_mul_f32 v21, v21, v248\n"
    "v_mfma_f32_16x16x32_bf16 v[40:43], a[176:179], v[180:183], v[40:43]\n"
    "v_mul_f32 v22, v22, v248\n"
    "v_mul_f32 v23, v23, v248\n"
    "v_mfma_f32_16x16x32_bf16 v[40:43], a[232:235], v[184:187], v[40:43]\n"
    "v_cvt_pk_bf16_f32 v16, v16, v17\n"
    "v_cvt_pk_bf16_f32 v17, v18, v19\n"
    "v_mfma_f32_16x16x32_bf16 v[44:47], a[180:183], v[180:183], v[44:47]\n"
    "v_cvt_pk_bf16_f32 v18, v20, v21\n"
    "v_cvt_pk_bf16_f32 v19, v22, v23\n"
    "v_mfma_f32_16x16x32_bf16 v[44:47], a[236:239], v[184:187], v[44:47]\n"
    "buffer_store_dwordx4 v[16:19], v240, s[64:67], s48 offen offset:64 sc1\n"
    "v_mfma_f32_16x16x32_bf16 v[48:51], a[184:187], v[180:183], v[48:51]\n"
    "v_mul_f32 v24, v24, v248\n"
    "v_mul_f32 v25, v25, v248\n"
    "v_mfma_f32_16x16x32_bf16 v[48:51], a[240:243], v[184:187], v[48:51]\n"
    "v_mul_f32 v26, v26, v248\n"
    "v_mul_f32 v27, v27, v248\n"
    "v_mfma_f32_16x16x32_bf16 v[52:55], a[188:191], v[180:183], v[52:55]\n"
    "v_mul_f32 v28, v28, v248\n"
    "v_mul_f32 v29, v29, v248\n"
    "v_mfma_f32_16x16x32_bf16 v[52:55], a[244:247], v[184:187], v[52:55]\n"
    "v_mul_f32 v30, v30, v248\n"
    "v_mul_f32 v31, v31, v248\n"
    "v_mfma_f32_16x16x32_bf16 v[56:59], a[192:195], v[180:183], v[56:59]\n"
    "v_cvt_pk_bf16_f32 v24, v24, v25\n"
    "v_cvt_pk_bf16_f32 v25, v26, v27\n"
    "v_mfma_f32_16x16x32_bf16 v[56:59], a[248:251], v[184:187], v[56:59]\n"
    "v_cvt_pk_bf16_f32 v26, v28, v29\n"
    "v_cvt_pk_bf16_f32 v27, v30, v31\n"
    "v_mfma_f32_16x16x32_bf16 v[60:63], a[196:199], v[180:183], v[60:63]\n"
    "v_accvgpr_read_b32 v254, a212\n"
    "v_mul_f32 v32, v32, v248\n"
    "v_mfma_f32_16x16x32_bf16 v[60:63], a[252:255], v[184:187], v[60:63]\n"
    "v_rcp_f32 v249, v254\n"
    "v_mfma_f32_16x16x32_bf16 v[64:67], a[200:203], v[180:183], v[64:67]\n"
    "buffer_store_dwordx4 v[24:27], v240, s[64:67], s48 offen offset:128 sc1\n"
    "v_mfma_f32_16x16x32_bf16 v[64:67], a[32:35], v[184:187], v[64:67]\n"
    "v_cmp_nge_f32 vcc, v254, v246\n"
    "v_mul_f32 v33, v33, v248\n"
    "v_mfma_f32_16x16x32_bf16 v[68:71], a[204:207], v[180:183], v[68:71]\n"
    "s_or_b64 s[52:53], s[52:53], vcc\n"
    "v_mul_f32 v34, v34, v248\n"
    "v_mul_f32 v35, v35, v248\n"
    "v_mfma_f32_16x16x32_bf16 v[68:71], a[36:39], v[184:187], v[68:71]\n"
    "v_cmp_nle_f32 vcc, v254, v247\n"
    "v_mul_f32 v36, v36, v248\n"
    "v_mfma_f32_16x16x32_bf16 v[216:219], a[112:115], a[64:67], 0\n"
    "s_or_b64 s[52:53], s[52:53], vcc\n"
    "v_mul_f32 v37, v37, v248\n"
    "v_mul_f32 v38, v38, v248\n"
    "v_mfma_f32_16x16x32_bf16 v[216:219], a[116:119], a[68:71], v[216:219]\n"
    "v_mul_f32 v39, v39, v248\n"
    "v_cvt_pk_bf16_f32 v32, v32, v33\n"
    "v_mfma_f32_16x16x32_bf16 v[216:219], a[120:123], a[72:75], v[216:219]\n"
    "v_cvt_pk_bf16_f32 v33, v34, v35\n"
    "v_cvt_pk_bf16_f32 v34, v36, v37\n"
    "v_mfma_f32_16x16x32_bf16 v[216:219], a[124:127], a[76:79], v[216:219]\n"
    "ds_read_b64_tr_b16 a[176:177], v4 offset:0\n"
    "v_cvt_pk_bf16_f32 v35, v38, v39\n"
    "v_mfma_f32_16x16x32_bf16 v[220:223], a[128:131], a[64:67], 0\n"
    "ds_read_b64_tr_b16 a[178:179], v4 offset:4096\n"
    "ds_read_b64_tr_b16 a[180:181], v6 offset:0\n"
    "v_mfma_f32_16x16x32_bf16 v[220:223], a[132:135], a[68:71], v[220:223]\n"
    "buffer_store_dwordx4 v[32:35], v240, s[64:67], s48 offen offset:192 sc1\n"
    "v_mfma_f32_16x16x32_bf16 v[220:223], a[136:139], a[72:75], v[220:223]\n"
    "ds_read_b64_tr_b16 a[182:183], v6 offset:4096\n"
    "ds_read_b64_tr_b16 a[184:185], v4 offset:8\n"
    "v_mfma_f32_16x16x32_bf16 v[220:223], a[140:143], a[76:79], v[220:223]\n"
    "s_waitcnt lgkmcnt(0)\n"
    "ds_read_b64_tr_b16 a[186:187], v4 offset:4104\n"
    "ds_read_b64_tr_b16 a[188:189], v6 offset:8\n"
    "ds_read_b64_tr_b16 a[190:191], v6 offset:4104\n"
    "v_mul_f32 v40, v40, v249\n"
    "v_mul_f32 v41, v41, v249\n"
    "v_mul_f32 v42, v42, v249\n"
    "v_mul_f32 v43, v43, v249\n"
    "v_mul_f32 v44, v44, v249\n"
    "v_mul_f32 v45, v45, v249\n"
    "v_mul_f32 v46, v46, v249\n"
    "v_mul_f32 v47, v47, v249\n"
    "v_cvt_pk_bf16_f32 v40, v40, v41\n"
    "v_cvt_pk_bf16_f32 v41, v42, v43\n"
    "v_cvt_pk_bf16_f32 v42, v44, v45\n"
    "v_mfma_f32_16x16x32_bf16 v[72:75], a[176:179], v[152:155], v[72:75]\n"
    "v_cvt_pk_bf16_f32 v43, v46, v47\n"
    "v_mul_f32 v48, v48, v249\n"
    "buffer_store_dwordx4 v[40:43], v240, s[64:67], s49 offen offset:0 sc1\n"
    "v_mul_f32 v49, v49, v249\n"
    "v_mul_f32 v50, v50, v249\n"
    "v_mfma_f32_16x16x32_bf16 v[72:75], a[180:183], v[156:159], v[72:75]\n"
    "s_waitcnt lgkmcnt(0)\n"
    "v_mul_f32 v51, v51, v249\n"
    "v_mul_f32 v52, v52, v249\n"
    "v_mfma_f32_16x16x32_bf16 v[76:79], a[184:187], v[152:155], v[76:79]\n"
    "v_mul_f32 v53, v53, v249\n"
    "v_mul_f32 v54, v54, v249\n"
    "v_mfma_f32_16x16x32_bf16 v[76:79], a[188:191], v[156:159], v[76:79]\n"
    "v_mul_f32 v55, v55, v249\n"
    "v_cvt_pk_bf16_f32 v48, v48, v49\n"
    "v_mfma_f32_16x16x32_bf16 a[0:3], a[176:179], v[168:171], a[0:3]\n"
    "v_cvt_pk_bf16_f32 v49, v50, v51\n"
    "v_cvt_pk_bf16_f32 v50, v52, v53\n"
    "v_mfma_f32_16x16x32_bf16 a[216:219], a[228:231], v[152:155], a[216:219]\n"
    "v_cvt_pk_bf16_f32 v51, v54, v55\n"
    "v_mul_f32 v56, v56, v249\n"
    "v_mfma_f32_16x16x32_bf16 a[216:219], a[228:231], v[156:159], a[216:219]\n"
    "buffer_store_dwordx4 v[48:51], v240, s[64:67], s49 offen offset:64 sc1\n"
    "v_mfma_f32_16x16x32_bf16 v[104:107], a[176:179], v[160:163], v[104:107]\n"
    "v_mul_f32 v57, v57, v249\n"
    "v_mul_f32 v58, v58, v249\n"
    "v_mfma_f32_16x16x32_bf16 v[104:107], a[180:183], v[164:167], v[104:107]\n"
    "v_mul_f32 v59, v59, v249\n"
    "v_mul_f32 v60, v60, v249\n"
    "v_mfma_f32_16x16x32_bf16 v[208:211], a[112:115], a[80:83], 0\n"
    "v_mul_f32 v61, v61, v249\n"
    "v_mul_f32 v62, v62, v249\n"
    "v_mfma_f32_16x16x32_bf16 v[208:211], a[116:119], a[84:87], v[208:211]\n"
    "v_mul_f32 v63, v63, v249\n"
    "v_cvt_pk_bf16_f32 v56, v56, v57\n"
    "v_mfma_f32_16x16x32_bf16 v[208:211], a[120:123], a[88:91], v[208:211]\n"
    "v_cvt_pk_bf16_f32 v57, v58, v59\n"
    "v_cvt_pk_bf16_f32 v58, v60, v61\n"
    "v_mfma_f32_16x16x32_bf16 v[208:211], a[124:127], a[92:95], v[208:211]\n"
    "v_cvt_pk_bf16_f32 v59, v62, v63\n"
    "v_mul_f32 v64, v64, v249\n"
    "v_mfma_f32_16x16x32_bf16 v[212:215], a[128:131], a[80:83], 0\n"
    "buffer_store_dwordx4 v[56:59], v240, s[64:67], s49 offen offset:128 sc1\n"
    "v_mfma_f32_16x16x32_bf16 v[212:215], a[132:135], a[84:87], v[212:215]\n"
    "v_mul_f32 v65, v65, v249\n"
    "v_mul_f32 v66, v66, v249\n"
    "v_mfma_f32_16x16x32_bf16 v[212:215], a[136:139], a[88:91], v[212:215]\n"
    "v_mul_f32 v67, v67, v249\n"
    "v_mul_f32 v68, v68, v249\n"
    "v_mfma_f32_16x16x32_bf16 v[212:215], a[140:143], a[92:95], v[212:215]\n"
    "v_mul_f32 v69, v69, v249\n"
    "v_mul_f32 v70, v70, v249\n"
    "v_mfma_f32_16x16x32_bf16 a[4:7], a[184:187], v[168:171], a[4:7]\n"
    "v_mul_f32 v71, v71, v249\n"
    "v_cvt_pk_bf16_f32 v64, v64, v65\n"
    "v_mfma_f32_16x16x32_bf16 v[108:111], a[184:187], v[160:163], v[108:111]\n"
    "v_cvt_pk_bf16_f32 v65, v66, v67\n"
    "v_cvt_pk_bf16_f32 v66, v68, v69\n"
    "v_mfma_f32_16x16x32_bf16 v[108:111], a[188:191], v[164:167], v[108:111]\n"
    "ds_read_b64_tr_b16 a[192:193], v5 offset:0\n"
    "v_cvt_pk_bf16_f32 v67, v70, v71\n"
    "v_mfma_f32_16x16x32_bf16 a[220:223], a[228:231], v[160:163], a[220:223]\n"
    "ds_read_b64_tr_b16 a[194:195], v5 offset:4096\n"
    "ds_read_b64_tr_b16 a[196:197], v7 offset:0\n"
    "v_mfma_f32_16x16x32_bf16 a[220:223], a[228:231], v[164:167], a[220:223]\n"
    "ds_read_b64_tr_b16 a[198:199], v7 offset:4096\n"
    "ds_read_b64_tr_b16 a[200:201], v5 offset:8\n"
    "v_mfma_f32_16x16x32_bf16 a[224:227], a[228:231], v[168:171], a[224:227]\n"
    "s_waitcnt lgkmcnt(0)\n"
    "buffer_store_dwordx4 v[64:67], v240, s[64:67], s49 offen offset:192 sc1\n"
    "ds_read_b64_tr_b16 a[202:203], v5 offset:4104\n"
    "ds_read_b64_tr_b16 a[204:205], v7 offset:8\n"
    "ds_read_b64_tr_b16 a[206:207], v7 offset:4104\n"
    "ds_read_b64_tr_b16 a[176:177], v4 offset:1024\n"
    "ds_read_b64_tr_b16 a[178:179], v4 offset:5120\n"
    "ds_read_b64_tr_b16 a[180:181], v6 offset:1024\n"
    "v_exp_f32 v216, v216\n"
    "ds_read_b64_tr_b16 a[182:183], v6 offset:5120\n"
    "ds_read_b64_tr_b16 a[184:185], v4 offset:1032\n"
    "v_exp_f32 v217, v217\n"
    "ds_read_b64_tr_b16 a[186:187], v4 offset:5128\n"
    "v_exp_f32 v218, v218\n"
    "v_cvt_pk_bf16_f32 v188, v216, v217\n"
    "v_mfma_f32_16x16x32_bf16 v[80:83], a[192:195], v[152:155], v[80:83]\n"
    "v_exp_f32 v219, v219\n"
    "v_mfma_f32_16x16x32_bf16 v[80:83], a[196:199], v[156:159], v[80:83]\n"
    "s_waitcnt lgkmcnt(1)\n"
    "ds_read_b64_tr_b16 a[188:189], v6 offset:1032\n"
    "v_cvt_pk_bf16_f32 v189, v218, v219\n"
    "ds_read_b64_tr_b16 a[190:191], v6 offset:5128\n"
    "v_exp_f32 v220, v220\n"
    "v_mfma_f32_16x16x32_bf16 v[84:87], a[200:203], v[152:155], v[84:87]\n"
    "v_exp_f32 v221, v221\n"
    "v_mfma_f32_16x16x32_bf16 v[84:87], a[204:207], v[156:159], v[84:87]\n"
    "v_exp_f32 v222, v222\n"
    "v_mfma_f32_16x16x32_bf16 v[216:219], a[112:115], a[96:99], 0\n"
    "v_cvt_pk_bf16_f32 v190, v220, v221\n"
    "v_mfma_f32_16x16x32_bf16 v[216:219], a[116:119], a[100:103], v[216:219]\n"
    "v_exp_f32 v223, v223\n"
    "v_mfma_f32_16x16x32_bf16 v[216:219], a[120:123], a[104:107], v[216:219]\n"
    "v_cvt_pk_bf16_f32 v191, v222, v223\n"
    "v_mfma_f32_16x16x32_bf16 v[216:219], a[124:127], a[108:111], v[216:219]\n"
    "v_exp_f32 v208, v208\n"
    "v_mfma_f32_16x16x32_bf16 v[220:223], a[128:131], a[96:99], 0\n"
    "v_exp_f32 v209, v209\n"
    "v_mfma_f32_16x16x32_bf16 v[220:223], a[132:135], a[100:103], v[220:223]\n"
    "v_exp_f32 v210, v210\n"
    "v_mfma_f32_16x16x32_bf16 v[220:223], a[136:139], a[104:107], v[220:223]\n"
    "v_cvt_pk_bf16_f32 v196, v208, v209\n"
    "v_mfma_f32_16x16x32_bf16 v[220:223], a[140:143], a[108:111], v[220:223]\n"
    "v_exp_f32 v211, v211\n"
    "v_mfma_f32_16x16x32_bf16 a[8:11], a[192:195], v[168:171], a[8:11]\n"
    "v_cvt_pk_bf16_f32 v197, v210, v211\n"
    "v_mfma_f32_16x16x32_bf16 v[112:115], a[192:195], v[160:163], v[112:115]\n"
    "v_exp_f32 v212, v212\n"
    "v_mfma_f32_16x16x32_bf16 v[112:115], a[196:199], v[164:167], v[112:115]\n"
    "v_exp_f32 v213, v213\n"
    "v_mfma_f32_16x16x32_bf16 v[116:119], a[200:203], v[160:163], v[116:119]\n"
    "v_exp_f32 v214, v214\n"
    "v_mfma_f32_16x16x32_bf16 v[116:119], a[204:207], v[164:167], v[116:119]\n"
    "ds_read_b64_tr_b16 a[192:193], v5 offset:1024\n"
    "v_cvt_pk_bf16_f32 v198, v212, v213\n"
    "v_mfma_f32_16x16x32_bf16 a[12:15], a[200:203], v[168:171], a[12:15]\n"
    "v_exp_f32 v215, v215\n"
    "v_mfma_f32_16x16x32_bf16 v[88:91], a[176:179], v[152:155], v[88:91]\n"
    "ds_read_b64_tr_b16 a[194:195], v5 offset:5120\n"
    "v_cvt_pk_bf16_f32 v199, v214, v215\n"
    "v_mfma_f32_16x16x32_bf16 v[88:91], a[180:183], v[156:159], v[88:91]\n"
    "ds_read_b64_tr_b16 a[196:197], v7 offset:1024\n"
    "ds_read_b64_tr_b16 a[198:199], v7 offset:5120\n"
    "v_mfma_f32_16x16x32_bf16 v[208:211], a[144:147], a[64:67], 0\n"
    "v_exp_f32 v216, v216\n"
    "v_mfma_f32_16x16x32_bf16 v[208:211], a[148:151], a[68:71], v[208:211]\n"
    "v_exp_f32 v217, v217\n"
    "v_mfma_f32_16x16x32_bf16 v[208:211], a[152:155], a[72:75], v[208:211]\n"
    "ds_read_b64_tr_b16 a[200:201], v5 offset:1032\n"
    "ds_read_b64_tr_b16 a[202:203], v5 offset:5128\n"
    "v_mfma_f32_16x16x32_bf16 v[208:211], a[156:159], a[76:79], v[208:211]\n"
    "v_exp_f32 v218, v218\n"
    "v_mfma_f32_16x16x32_bf16 v[212:215], a[160:163], a[64:67], 0\n"
    "ds_read_b64_tr_b16 a[204:205], v7 offset:1032\n"
    "v_cvt_pk_bf16_f32 v204, v216, v217\n"
    "v_mfma_f32_16x16x32_bf16 v[212:215], a[164:167], a[68:71], v[212:215]\n"
    "ds_read_b64_tr_b16 a[206:207], v7 offset:5128\n"
    "ds_read_b64_tr_b16 a[64:65], v7 offset:17408\n"
    "v_mfma_f32_16x16x32_bf16 v[212:215], a[168:171], a[72:75], v[212:215]\n"
    "v_exp_f32 v219, v219\n"
    "v_mfma_f32_16x16x32_bf16 v[212:215], a[172:175], a[76:79], v[212:215]\n"
    "s_waitcnt lgkmcnt(3)\n"
    "ds_read_b64_tr_b16 a[66:67], v7 offset:21504\n"
    "v_cvt_pk_bf16_f32 v205, v218, v219\n"
    "v_mfma_f32_16x16x32_bf16 v[92:95], a[184:187], v[152:155], v[92:95]\n"
    "v_exp_f32 v220, v220\n"
    "v_mfma_f32_16x16x32_bf16 v[92:95], a[188:191], v[156:159], v[92:95]\n"
    "v_exp_f32 v221, v221\n"
    "v_mfma_f32_16x16x32_bf16 a[16:19], a[176:179], v[168:171], a[16:19]\n"
    "v_exp_f32 v222, v222\n"
    "v_mfma_f32_16x16x32_bf16 v[120:123], a[176:179], v[160:163], v[120:123]\n"
    "ds_read_b64_tr_b16 a[68:69], v7 offset:17416\n"
    "v_cvt_pk_bf16_f32 v206, v220, v221\n"
    "v_mfma_f32_16x16x32_bf16 v[120:123], a[180:183], v[164:167], v[120:123]\n"
    "v_exp_f32 v223, v223\n"
    "v_mfma_f32_16x16x32_bf16 v[124:127], a[184:187], v[160:163], v[124:127]\n"
    "ds_read_b64_tr_b16 a[176:177], v4 offset:16384\n"
    "v_cvt_pk_bf16_f32 v207, v222, v223\n"
    "v_mfma_f32_16x16x32_bf16 v[124:127], a[188:191], v[164:167], v[124:127]\n"
    "ds_read_b64_tr_b16 a[178:179], v4 offset:20480\n"
    "ds_read_b64_tr_b16 a[180:181], v4 offset:16392\n"
    "v_mfma_f32_16x16x32_bf16 a[20:23], a[184:187], v[168:171], a[20:23]\n"
    "v_exp_f32 v208, v208\n"
    "v_mfma_f32_16x16x32_bf16 v[96:99], a[192:195], v[152:155], v[96:99]\n"
    "v_exp_f32 v209, v209\n"
    "v_mfma_f32_16x16x32_bf16 v[96:99], a[196:199], v[156:159], v[96:99]\n"
    "ds_read_b64_tr_b16 a[182:183], v4 offset:20488\n"
    "v_cvt_pk_bf16_f32 v192, v208, v209\n"
    "v_mfma_f32_16x16x32_bf16 v[216:219], a[144:147], a[80:83], 0\n"
    "v_exp_f32 v210, v210\n"
    "v_mfma_f32_16x16x32_bf16 v[216:219], a[148:151], a[84:87], v[216:219]\n"
    "v_exp_f32 v211, v211\n"
    "v_mfma_f32_16x16x32_bf16 v[216:219], a[152:155], a[88:91], v[216:219]\n"
    "ds_read_b64_tr_b16 a[184:185], v5 offset:16384\n"
    "ds_read_b64_tr_b16 a[186:187], v5 offset:20480\n"
    "v_mfma_f32_16x16x32_bf16 v[216:219], a[156:159], a[92:95], v[216:219]\n"
    "ds_read_b64_tr_b16 a[188:189], v5 offset:16392\n"
    "v_cvt_pk_bf16_f32 v193, v210, v211\n"
    "v_mfma_f32_16x16x32_bf16 v[220:223], a[160:163], a[80:83], 0\n"
    "v_exp_f32 v212, v212\n"
    "v_mfma_f32_16x16x32_bf16 v[220:223], a[164:167], a[84:87], v[220:223]\n"
    "v_exp_f32 v213, v213\n"
    "v_mfma_f32_16x16x32_bf16 v[220:223], a[168:171], a[88:91], v[220:223]\n"
    "ds_read_b64_tr_b16 a[190:191], v5 offset:20488\n"
    "v_cvt_pk_bf16_f32 v194, v212, v213\n"
    "v_mfma_f32_16x16x32_bf16 v[220:223], a[172:175], a[92:95], v[220:223]\n"
    "v_exp_f32 v214, v214\n"
    "v_mfma_f32_16x16x32_bf16 v[100:103], a[200:203], v[152:155], v[100:103]\n"
    "s_waitcnt lgkmcnt(1)\n"
    "v_exp_f32 v215, v215\n"
    "v_mfma_f32_16x16x32_bf16 v[100:103], a[204:207], v[156:159], v[100:103]\n"
    "ds_read_b64_tr_b16 a[70:71], v7 offset:21512\n"
    "v_cvt_pk_bf16_f32 v195, v214, v215\n"
    "v_mfma_f32_16x16x32_bf16 a[24:27], a[192:195], v[168:171], a[24:27]\n"
    "v_exp_f32 v216, v216\n"
    "v_mfma_f32_16x16x32_bf16 v[128:131], a[192:195], v[160:163], v[128:131]\n"
    "v_exp_f32 v217, v217\n"
    "v_mfma_f32_16x16x32_bf16 v[128:131], a[196:199], v[164:167], v[128:131]\n"
    "ds_read_b64_tr_b16 a[192:193], v4 offset:17408\n"
    "ds_read_b64_tr_b16 a[194:195], v4 offset:21504\n"
    "v_mfma_f32_16x16x32_bf16 v[132:135], a[200:203], v[160:163], v[132:135]\n"
    "ds_read_b64_tr_b16 a[196:197], v4 offset:17416\n"
    "ds_read_b64_tr_b16 a[198:199], v4 offset:21512\n"
    "v_mfma_f32_16x16x32_bf16 v[132:135], a[204:207], v[164:167], v[132:135]\n"
    "v_exp_f32 v218, v218\n"
    "v_mfma_f32_16x16x32_bf16 a[28:31], a[200:203], v[168:171], a[28:31]\n"
    "ds_read_b64_tr_b16 a[204:205], v5 offset:17416\n"
    "ds_read_b64_tr_b16 a[206:207], v5 offset:21512\n"
    "v_mfma_f32_16x16x32_bf16 a[216:219], a[228:231], v[188:191], a[216:219]\n"
    "ds_read_b64_tr_b16 a[200:201], v5 offset:17408\n"
    "ds_read_b64_tr_b16 a[202:203], v5 offset:21504\n"
    "v_mfma_f32_16x16x32_bf16 a[216:219], a[228:231], v[192:195], a[216:219]\n"
    "v_cvt_pk_bf16_f32 v200, v216, v217\n"
    "v_mfma_f32_16x16x32_bf16 v[72:75], a[176:179], v[188:191], v[72:75]\n"
    "v_exp_f32 v219, v219\n"
    "v_mfma_f32_16x16x32_bf16 v[72:75], a[232:235], v[192:195], v[72:75]\n"
    "v_cvt_pk_bf16_f32 v201, v218, v219\n"
    "v_mfma_f32_16x16x32_bf16 v[76:79], a[180:183], v[188:191], v[76:79]\n"
    "v_exp_f32 v220, v220\n"
    "v_mfma_f32_16x16x32_bf16 v[76:79], a[236:239], v[192:195], v[76:79]\n"
    "v_accvgpr_read_b32 v255, a216\n"
    "v_mfma_f32_16x16x32_bf16 v[80:83], a[184:187], v[188:191], v[80:83]\n"
    "v_rcp_f32 v250, v255\n"
    "v_mfma_f32_16x16x32_bf16 v[80:83], a[240:243], v[192:195], v[80:83]\n"
    "s_waitcnt lgkmcnt(0)\n"
    "v_cmp_nge_f32 vcc, v255, v246\n"
    "v_mul_f32 v72, v72, v250\n"
    "v_mfma_f32_16x16x32_bf16 v[84:87], a[188:191], v[188:191], v[84:87]\n"
    "s_or_b64 s[52:53], s[52:53], vcc\n"
    "v_mul_f32 v73, v73, v250\n"
    "v_mul_f32 v74, v74, v250\n"
    "v_mfma_f32_16x16x32_bf16 v[84:87], a[244:247], v[192:195], v[84:87]\n"
    "v_cmp_nle_f32 vcc, v255, v247\n"
    "v_mul_f32 v75, v75, v250\n"
    "v_mfma_f32_16x16x32_bf16 v[88:91], a[192:195], v[188:191], v[88:91]\n"
    "s_or_b64 s[52:53], s[52:53], vcc\n"
    "v_mul_f32 v76, v76, v250\n"
    "v_mul_f32 v77, v77, v250\n"
    "v_mfma_f32_16x16x32_bf16 v[88:91], a[248:251], v[192:195], v[88:91]\n"
    "v_mul_f32 v78, v78, v250\n"
    "v_mul_f32 v79, v79, v250\n"
    "v_mfma_f32_16x16x32_bf16 v[92:95], a[196:199], v[188:191], v[92:95]\n"
    "v_cvt_pk_bf16_f32 v72, v72, v73\n"
    "v_cvt_pk_bf16_f32 v73, v74, v75\n"
    "v_mfma_f32_16x16x32_bf16 v[92:95], a[252:255], v[192:195], v[92:95]\n"
    "v_cvt_pk_bf16_f32 v74, v76, v77\n"
    "v_cvt_pk_bf16_f32 v75, v78, v79\n"
    "v_mfma_f32_16x16x32_bf16 v[96:99], a[200:203], v[188:191], v[96:99]\n"
    "buffer_store_dwordx4 v[72:75], v240, s[64:67], s50 offen offset:0 sc1\n"
    "v_mfma_f32_16x16x32_bf16 v[96:99], a[64:67], v[192:195], v[96:99]\n"
    "v_mul_f32 v80, v80, v250\n"
    "v_mul_f32 v81, v81, v250\n"
    "v_mfma_f32_16x16x32_bf16 v[100:103], a[204:207], v[188:191], v[100:103]\n"
    "v_mul_f32 v82, v82, v250\n"
    "v_mul_f32 v83, v83, v250\n"
    "v_mfma_f32_16x16x32_bf16 v[100:103], a[68:71], v[192:195], v[100:103]\n"
    "v_mul_f32 v84, v84, v250\n"
    "v_mul_f32 v85, v85, v250\n"
    "v_mfma_f32_16x16x32_bf16 a[0:3], a[176:179], v[204:207], a[0:3]\n"
    "v_mul_f32 v86, v86, v250\n"
    "v_mul_f32 v87, v87, v250\n"
    "v_mfma_f32_16x16x32_bf16 a[4:7], a[180:183], v[204:207], a[4:7]\n"
    "v_cvt_pk_bf16_f32 v80, v80, v81\n"
    "v_cvt_pk_bf16_f32 v81, v82, v83\n"
    "v_mfma_f32_16x16x32_bf16 a[8:11], a[184:187], v[204:207], a[8:11]\n"
    "v_cvt_pk_bf16_f32 v82, v84, v85\n"
    "v_cvt_pk_bf16_f32 v83, v86, v87\n"
    "v_mfma_f32_16x16x32_bf16 a[12:15], a[188:191], v[204:207], a[12:15]\n"
    "buffer_store_dwordx4 v[80:83], v240, s[64:67], s50 offen offset:64 sc1\n"
    "v_mfma_f32_16x16x32_bf16 a[16:19], a[192:195], v[204:207], a[16:19]\n"
    "v_exp_f32 v221, v221\n"
    "v_mfma_f32_16x16x32_bf16 a[20:23], a[196:199], v[204:207], a[20:23]\n"
    "v_exp_f32 v222, v222\n"
    "v_mfma_f32_16x16x32_bf16 a[24:27], a[200:203], v[204:207], a[24:27]\n"
    "v_cvt_pk_bf16_f32 v202, v220, v221\n"
    "v_mul_f32 v88, v88, v250\n"
    "v_mfma_f32_16x16x32_bf16 a[28:31], a[204:207], v[204:207], a[28:31]\n"
    "v_mul_f32 v89, v89, v250\n"
    "v_mul_f32 v90, v90, v250\n"
    "v_mfma_f32_16x16x32_bf16 a[224:227], a[228:231], v[204:207], a[224:227]\n"
    "v_mul_f32 v91, v91, v250\n"
    "v_mul_f32 v92, v92, v250\n"
    "v_mfma_f32_16x16x32_bf16 a[220:223], a[228:231], v[196:199], a[220:223]\n"
    "v_mul_f32 v93, v93, v250\n"
    "v_mul_f32 v94, v94, v250\n"
    "v_mul_f32 v95, v95, v250\n"
    "v_exp_f32 v223, v223\n"
    "v_cvt_pk_bf16_f32 v88, v88, v89\n"
    "v_cvt_pk_bf16_f32 v89, v90, v91\n"
    "v_cvt_pk_bf16_f32 v90, v92, v93\n"
    "v_cvt_pk_bf16_f32 v91, v94, v95\n"
    "v_cvt_pk_bf16_f32 v203, v222, v223\n"
    "s_nop 1\n"
    "v_mfma_f32_16x16x32_bf16 a[220:223], a[228:231], v[200:203], a[220:223]\n"
    "buffer_store_dwordx4 v[88:91], v240, s[64:67], s50 offen offset:128 sc1\n"
    "v_mfma_f32_16x16x32_bf16 v[104:107], a[176:179], v[196:199], v[104:107]\n"
    "v_mul_f32 v96, v96, v250\n"
    "v_mul_f32 v97, v97, v250\n"
    "v_mfma_f32_16x16x32_bf16 v[104:107], a[232:235], v[200:203], v[104:107]\n"
    "v_mul_f32 v98, v98, v250\n"
    "v_mul_f32 v99, v99, v250\n"
    "v_mfma_f32_16x16x32_bf16 v[108:111], a[180:183], v[196:199], v[108:111]\n"
    "v_mul_f32 v100, v100, v250\n"
    "v_mul_f32 v101, v101, v250\n"
    "v_mfma_f32_16x16x32_bf16 v[108:111], a[236:239], v[200:203], v[108:111]\n"
    "v_mul_f32 v102, v102, v250\n"
    "v_mul_f32 v103, v103, v250\n"
    "v_mfma_f32_16x16x32_bf16 v[112:115], a[184:187], v[196:199], v[112:115]\n"
    "ds_write_b128 v242, a[0:3] offset:0\n"
    "v_mfma_f32_16x16x32_bf16 v[112:115], a[240:243], v[200:203], v[112:115]\n"
    "v_cvt_pk_bf16_f32 v96, v96, v97\n"
    "v_cvt_pk_bf16_f32 v97, v98, v99\n"
    "v_mfma_f32_16x16x32_bf16 v[116:119], a[188:191], v[196:199], v[116:119]\n"
    "v_cvt_pk_bf16_f32 v98, v100, v101\n"
    "v_cvt_pk_bf16_f32 v99, v102, v103\n"
    "v_mfma_f32_16x16x32_bf16 v[116:119], a[244:247], v[200:203], v[116:119]\n"
    "ds_write_b128 v242, a[4:7] offset:1024\n"
    "v_mfma_f32_16x16x32_bf16 v[120:123], a[192:195], v[196:199], v[120:123]\n"
    "ds_write_b128 v242, a[8:11] offset:2048\n"
    "v_mfma_f32_16x16x32_bf16 v[120:123], a[248:251], v[200:203], v[120:123]\n"
    "buffer_store_dwordx4 v[96:99], v240, s[64:67], s50 offen offset:192 sc1\n"
    "v_mfma_f32_16x16x32_bf16 v[124:127], a[196:199], v[196:199], v[124:127]\n"
    "ds_write_b128 v242, a[12:15] offset:3072\n"
    "v_mfma_f32_16x16x32_bf16 v[124:127], a[252:255], v[200:203], v[124:127]\n"
    "ds_write_b128 v242, a[16:19] offset:4096\n"
    "v_mfma_f32_16x16x32_bf16 v[128:131], a[200:203], v[196:199], v[128:131]\n"
    "ds_write_b128 v242, a[20:23] offset:5120\n"
    "v_mfma_f32_16x16x32_bf16 v[128:131], a[64:67], v[200:203], v[128:131]\n"
    "ds_write_b128 v242, a[24:27] offset:6144\n"
    "v_mfma_f32_16x16x32_bf16 v[132:135], a[204:207], v[196:199], v[132:135]\n"
    "ds_write_b128 v242, a[28:31] offset:7168\n"
    "v_mfma_f32_16x16x32_bf16 v[132:135], a[68:71], v[200:203], v[132:135]\n"
    "ds_write_b32 v242, a224 offset:8192\n"
    "v_accvgpr_read_b32 v253, a224\n"
    "s_waitcnt lgkmcnt(0)\n"
    "s_barrier\n"
    "ds_read_b128 v[136:139], v243 offset:0\n"
    "ds_read_b128 v[152:155], v244 offset:0\n"
    "ds_read_b32 v254, v245 offset:8192\n"
    "ds_read_b128 v[140:143], v243 offset:1024\n"
    "ds_read_b128 v[156:159], v244 offset:1024\n"
    "ds_read_b128 v[144:147], v243 offset:2048\n"
    "ds_read_b128 v[160:163], v244 offset:2048\n"
    "ds_read_b128 v[148:151], v243 offset:3072\n"
    "ds_read_b128 v[164:167], v244 offset:3072\n"
    "s_waitcnt lgkmcnt(6)\n"
    "v_add_f32 v253, v253, v254\n"
    "v_add_f32 v136, v136, v152\n"
    "v_add_f32 v137, v137, v153\n"
    "v_add_f32 v138, v138, v154\n"
    "v_accvgpr_read_b32 v254, a220\n"
    "v_add_f32 v139, v139, v155\n"
    "s_waitcnt lgkmcnt(4)\n"
    "v_add_f32 v140, v140, v156\n"
    "v_add_f32 v141, v141, v157\n"
    "v_rcp_f32 v251, v254\n"
    "v_add_f32 v142, v142, v158\n"
    "v_add_f32 v143, v143, v159\n"
    "s_waitcnt lgkmcnt(2)\n"
    "v_add_f32 v144, v144, v160\n"
    "v_add_f32 v145, v145, v161\n"
    "v_add_f32 v146, v146, v162\n"
    "v_add_f32 v147, v147, v163\n"
    "s_waitcnt lgkmcnt(0)\n"
    "v_add_f32 v148, v148, v164\n"
    "v_add_f32 v149, v149, v165\n"
    "v_add_f32 v150, v150, v166\n"
    "v_add_f32 v151, v151, v167\n"
    "v_rcp_f32 v252, v253\n"
    "v_cmp_nge_f32 vcc, v253, v246\n"
    "v_mul_f32 v104, v104, v251\n"
    "s_or_b64 s[52:53], s[52:53], vcc\n"
    "v_mul_f32 v136, v136, v252\n"
    "v_mul_f32 v137, v137, v252\n"
    "v_mul_f32 v138, v138, v252\n"
    "v_mul_f32 v139, v139, v252\n"
    "v_cmp_nle_f32 vcc, v253, v247\n"
    "v_mul_f32 v140, v140, v252\n"
    "v_mul_f32 v141, v141, v252\n"
    "v_mul_f32 v142, v142, v252\n"
    "s_or_b64 s[52:53], s[52:53], vcc\n"
    "v_mul_f32 v143, v143, v252\n"
    "v_cvt_pk_bf16_f32 v136, v136, v137\n"
    "v_cvt_pk_bf16_f32 v137, v138, v139\n"
    "v_cvt_pk_bf16_f32 v138, v140, v141\n"
    "v_cmp_nge_f32 vcc, v254, v246\n"
    "v_cvt_pk_bf16_f32 v139, v142, v143\n"
    "v_mul_f32 v144, v144, v252\n"
    "v_mul_f32 v145, v145, v252\n"
    "s_or_b64 s[52:53], s[52:53], vcc\n"
    "buffer_store_dwordx4 v[136:139], v241, s[64:67], 0 offen offset:0 sc1\n"
    "v_mul_f32 v146, v146, v252\n"
    "v_mul_f32 v147, v147, v252\n"
    "v_cmp_nle_f32 vcc, v254, v247\n"
    "v_mul_f32 v148, v148, v252\n"
    "v_mul_f32 v149, v149, v252\n"
    "v_mul_f32 v150, v150, v252\n"
    "s_or_b64 s[52:53], s[52:53], vcc\n"
    "v_mul_f32 v151, v151, v252\n"
    "v_cvt_pk_bf16_f32 v144, v144, v145\n"
    "v_cvt_pk_bf16_f32 v145, v146, v147\n"
    "v_cvt_pk_bf16_f32 v146, v148, v149\n"
    "s_cmp_lg_u64 s[52:53], 0\n"
    "v_cvt_pk_bf16_f32 v147, v150, v151\n"
    "v_mul_f32 v105, v105, v251\n"
    "v_mul_f32 v106, v106, v251\n"
    "v_mul_f32 v107, v107, v251\n"
    "s_cselect_b32 s58, 1, 0\n"
    "buffer_store_dwordx4 v[144:147], v241, s[64:67], 0 offen offset:64 sc1\n"
    "v_mul_f32 v108, v108, v251\n"
    "v_mul_f32 v109, v109, v251\n"
    "v_mov_b32 v1, s58\n"
    "v_mul_f32 v110, v110, v251\n"
    "v_mul_f32 v111, v111, v251\n"
    "v_cvt_pk_bf16_f32 v104, v104, v105\n"
    "ds_write_b32 v2, v1\n"
    "v_cvt_pk_bf16_f32 v105, v106, v107\n"
    "v_cvt_pk_bf16_f32 v106, v108, v109\n"
    "v_cvt_pk_bf16_f32 v107, v110, v111\n"
    "s_waitcnt lgkmcnt(0)\n"
    "s_barrier\n"
    "ds_read_b128 v[8:11], v3\n"
    "buffer_store_dwordx4 v[104:107], v240, s[64:67], s51 offen offset:0 sc1\n"
    "v_mul_f32 v112, v112, v251\n"
    "v_mul_f32 v113, v113, v251\n"
    "v_mul_f32 v114, v114, v251\n"
    "v_mul_f32 v115, v115, v251\n"
    "v_mul_f32 v116, v116, v251\n"
    "v_mul_f32 v117, v117, v251\n"
    "v_mul_f32 v118, v118, v251\n"
    "v_mul_f32 v119, v119, v251\n"
    "v_cvt_pk_bf16_f32 v112, v112, v113\n"
    "v_cvt_pk_bf16_f32 v113, v114, v115\n"
    "v_cvt_pk_bf16_f32 v114, v116, v117\n"
    "v_cvt_pk_bf16_f32 v115, v118, v119\n"
    "v_mul_f32 v120, v120, v251\n"
    "buffer_store_dwordx4 v[112:115], v240, s[64:67], s51 offen offset:64 sc1\n"
    "v_mul_f32 v121, v121, v251\n"
    "v_mul_f32 v122, v122, v251\n"
    "v_mul_f32 v123, v123, v251\n"
    "v_mul_f32 v124, v124, v251\n"
    "v_mul_f32 v125, v125, v251\n"
    "v_mul_f32 v126, v126, v251\n"
    "v_mul_f32 v127, v127, v251\n"
    "v_cvt_pk_bf16_f32 v120, v120, v121\n"
    "v_cvt_pk_bf16_f32 v121, v122, v123\n"
    "v_cvt_pk_bf16_f32 v122, v124, v125\n"
    "v_cvt_pk_bf16_f32 v123, v126, v127\n"
    "v_mul_f32 v128, v128, v251\n"
    "v_mul_f32 v129, v129, v251\n"
    "v_mul_f32 v130, v130, v251\n"
    "buffer_store_dwordx4 v[120:123], v240, s[64:67], s51 offen offset:128 sc1\n"
    "v_mul_f32 v131, v131, v251\n"
    "v_mul_f32 v132, v132, v251\n"
    "v_mul_f32 v133, v133, v251\n"
    "v_mul_f32 v134, v134, v251\n"
    "v_mul_f32 v135, v135, v251\n"
    "v_cvt_pk_bf16_f32 v128, v128, v129\n"
    "v_cvt_pk_bf16_f32 v129, v130, v131\n"
    "v_cvt_pk_bf16_f32 v130, v132, v133\n"
    "v_cvt_pk_bf16_f32 v131, v134, v135\n"
    "buffer_store_dwordx4 v[128:131], v240, s[64:67], s51 offen offset:192 sc1\n"
    "s_waitcnt lgkmcnt(0)\n"
    "v_or_b32 v8, v8, v9\n"
    "v_or3_b32 v8, v8, v10, v11\n"
    "s_nop 1\n"
    "v_readfirstlane_b32 %[flag], v8\n"
    "s_nop 3\n"
    : [flag] "=s"(flag)
    : [qlo] "s"((unsigned)(size_t)Q), [qhi] "s"((unsigned)((size_t)Q >> 32)), [klo] "s"((unsigned)(size_t)K), [khi] "s"((unsigned)((size_t)K >> 32)), [vlo] "s"((unsigned)(size_t)V), [vhi] "s"((unsigned)((size_t)V >> 32)), [olo] "s"((unsigned)(size_t)O), [ohi] "s"((unsigned)((size_t)O >> 32)), [bh] "s"(bh), [lds] "s"((unsigned)(size_t)lds), [tid] "{v0}"(threadIdx.x)
    :
      "v1", "v2", "v3", "v4", "v5", "v6", "v7", "v8", "v9", "v10", "v11", "v12", "v13", "v14", "v15", "v16", "v17", "v18", "v19", "v20", "v21", "v22", "v23", "v24",
      "v25", "v26", "v27", "v28", "v29", "v30", "v31", "v32", "v33", "v34", "v35", "v36", "v37", "v38", "v39", "v40", "v41", "v42", "v43", "v44", "v45", "v46", "v47", "v48",
      "v49", "v50", "v51", "v52", "v53", "v54", "v55", "v56", "v57", "v58", "v59", "v60", "v61", "v62", "v63", "v64", "v65", "v66", "v67", "v68", "v69", "v70", "v71", "v72",
      "v73", "v74", "v75", "v76", "v77", "v78", "v79", "v80", "v81", "v82", "v83", "v84", "v85", "v86", "v87", "v88", "v89", "v90", "v91", "v92", "v93", "v94", "v95", "v96",
      "v97", "v98", "v99", "v100", "v101", "v102", "v103", "v104", "v105", "v106", "v107", "v108", "v109", "v110", "v111", "v112", "v113", "v114", "v115", "v116", "v117", "v118", "v119", "v120",
      "v121", "v122", "v123", "v124", "v125", "v126", "v127", "v128", "v129", "v130", "v131", "v132", "v133", "v134", "v135", "v136", "v137", "v138", "v139", "v140", "v141", "v142", "v143", "v144",
      "v145", "v146", "v147", "v148", "v149", "v150", "v151", "v152", "v153", "v154", "v155", "v156", "v157", "v158", "v159", "v160", "v161", "v162", "v163", "v164", "v165", "v166", "v167", "v168",
      "v169", "v170", "v171", "v172", "v173", "v174", "v175", "v176", "v177", "v178", "v179", "v180", "v181", "v182", "v183", "v184", "v185", "v186", "v187", "v188", "v189", "v190", "v191", "v192",
      "v193", "v194", "v195", "v196", "v197", "v198", "v199", "v200", "v201", "v202", "v203", "v204", "v205", "v206", "v207", "v208", "v209", "v210", "v211", "v212", "v213", "v214", "v215", "v216",
      "v217", "v218", "v219", "v220", "v221", "v222", "v223", "v224", "v225", "v226", "v227", "v228", "v229", "v230", "v231", "v232", "v233", "v234", "v235", "v236", "v237", "v238", "v239", "v240",
      "v241", "v242", "v243", "v244", "v245", "v246", "v247", "v248", "v249", "v250", "v251", "v252", "v253", "v254", "v255", "a0", "a1", "a2", "a3", "a4", "a5", "a6", "a7", "a8",
      "a9", "a10", "a11", "a12", "a13", "a14", "a15", "a16", "a17", "a18", "a19", "a20", "a21", "a22", "a23", "a24", "a25", "a26", "a27", "a28", "a29", "a30", "a31", "a32",
      "a33", "a34", "a35", "a36", "a37", "a38", "a39", "a40", "a41", "a42", "a43", "a44", "a45", "a46", "a47", "a48", "a49", "a50", "a51", "a52", "a53", "a54", "a55", "a56",
      "a57", "a58", "a59", "a60", "a61", "a62", "a63", "a64", "a65", "a66", "a67", "a68", "a69", "a70", "a71", "a72", "a73", "a74", "a75", "a76", "a77", "a78", "a79", "a80",
      "a81", "a82", "a83", "a84", "a85", "a86", "a87", "a88", "a89", "a90", "a91", "a92", "a93", "a94", "a95", "a96", "a97", "a98", "a99", "a100", "a101", "a102", "a103", "a104",
      "a105", "a106", "a107", "a108", "a109", "a110", "a111", "a112", "a113", "a114", "a115", "a116", "a117", "a118", "a119", "a120", "a121", "a122", "a123", "a124", "a125", "a126", "a127", "a128",
      "a129", "a130", "a131", "a132", "a133", "a134", "a135", "a136", "a137", "a138", "a139", "a140", "a141", "a142", "a143", "a144", "a145", "a146", "a147", "a148", "a149", "a150", "a151", "a152",
      "a153", "a154", "a155", "a156", "a157", "a158", "a159", "a160", "a161", "a162", "a163", "a164", "a165", "a166", "a167", "a168", "a169", "a170", "a171", "a172", "a173", "a174", "a175", "a176",
      "a177", "a178", "a179", "a180", "a181", "a182", "a183", "a184", "a185", "a186", "a187", "a188", "a189", "a190", "a191", "a192", "a193", "a194", "a195", "a196", "a197", "a198", "a199", "a200",
      "a201", "a202", "a203", "a204", "a205", "a206", "a207", "a208", "a209", "a210", "a211", "a212", "a213", "a214", "a215", "a216", "a217", "a218", "a219", "a220", "a221", "a222", "a223", "a224",
      "a225", "a226", "a227", "a228", "a229", "a230", "a231", "a232", "a233", "a234", "a235", "a236", "a237", "a238", "a239", "a240", "a241", "a242", "a243", "a244", "a245", "a246", "a247", "a248",
      "a249", "a250", "a251", "a252", "a253", "a254", "a255", "s12", "s13", "s14", "s15", "s16", "s20", "s21", "s22", "s23", "s24", "s25", "s26", "s27", "s28", "s29", "s30", "s31",
      "s36", "s37", "s38", "s39", "s40", "s41", "s42", "s43", "s44", "s45", "s46", "s47", "s48", "s49", "s50", "s51", "s52", "s53", "s54", "s55", "s56", "s57", "s58", "s59",
      "s60", "s61", "s62", "s63", "s64", "s65", "s66", "s67", "s68", "s69", "s70", "s71", "s72", "s73", "s74", "s75", "vcc", "scc", "memory");

  if (flag) {
    asm volatile("s_waitcnt vmcnt(0)" ::: "memory");
    __syncthreads();
    fallback_head(Q + (size_t)bh * kSQ * kD, K + (size_t)bh * kSK * kD, V + (size_t)bh * kSK * kD, O + (size_t)bh * kSQ * kD, lds);
  }
}

extern "C" void kernel_launch(void* const* d_in, const int* in_sizes, int n_in, void* d_out, int out_size, void* d_ws, size_t ws_size, hipStream_t stream) {
  (void)in_sizes; (void)n_in; (void)out_size; (void)d_ws; (void)ws_size;
  (void)hipFuncSetAttribute((const void*)gqa_softmax_attn, hipFuncAttributeMaxDynamicSharedMemorySize, kLds);
  hipLaunchKernelGGL(gqa_softmax_attn, dim3(kB * kH), dim3(256), kLds, stream,
                     (const u16*)d_in[0], (const u16*)d_in[1], (const u16*)d_in[2], (u16*)d_out);
}
```
